```python
import torch

def _softmax(x, axis=-1):
    return torch.softmax(x, dim=axis)

def _gelu(x):
    return torch.nn.functional.gelu(x, approximate='tanh')

def _silu(x):
    return torch.nn.functional.silu(x)

def _sigmoid(x):
    return torch.sigmoid(x)

def _layernorm(x, axis=-1, eps=1e-05):
    m = x.mean(dim=axis, keepdim=True)
    v = x.var(dim=axis, keepdim=True, unbiased=False)
    return (x - m) / torch.sqrt(v + eps)

def _scatter_add(tgt, idx, src, axis=0):
    return tgt.clone().index_add_(axis, idx, src)

def _segment_sum(src, seg, n):
    out = torch.zeros((n,) + src.shape[1:], dtype=src.dtype)
    return out.index_add_(0, seg, src)

def _pad(x, pw):
    flat = tuple((v for pair in reversed(pw) for v in pair))
    return torch.nn.functional.pad(x, flat)
kernel_name = 'adaLN_cond__mha_block__glu_gated_glu__prompt'

def setup_inputs():

    _w = lambda a: (lambda t: t.to(torch.bfloat16) if t.is_floating_point() else t)(torch.as_tensor(a))

    def _A_setup():
        B, S, D, H = (1, 2048, 512, 32)
        return {'x': _w((lambda *s: torch.randn(*s, dtype=torch.bfloat16) * 0.05)(B, S, D)), 'wqkv': _w((lambda *s: torch.randn(*s, dtype=torch.bfloat16) * 0.05)(D, 3 * D)), 'wo': _w((lambda *s: torch.randn(*s, dtype=torch.bfloat16) * 0.05)(D, D))}
    _A_in = _A_setup()

    def _B_setup():
        B, S, D, F = (1, 2048, 512, 1376)
        return {'x': _w((lambda *s: torch.randn(*s, dtype=torch.bfloat16) * 0.05)(B, S, D)), 'w_gate': _w((lambda *s: torch.randn(*s, dtype=torch.bfloat16) * 0.05)(D, F)), 'w_up': _w((lambda *s: torch.randn(*s, dtype=torch.bfloat16) * 0.05)(D, F)), 'w_down': _w((lambda *s: torch.randn(*s, dtype=torch.bfloat16) * 0.05)(F, D))}
    _B_in = _B_setup()
    return {'A_x': _A_in['x'], 'A_wqkv': _A_in['wqkv'], 'A_wo': _A_in['wo'], 'B_x': _B_in['x'], 'B_w_gate': _B_in['w_gate'], 'B_w_up': _B_in['w_up'], 'B_w_down': _B_in['w_down'], 'c': _w((lambda *s: torch.randn(*s, dtype=torch.bfloat16) * 0.1)(1, 512) * 8.0), 'w_mod': _w((lambda *s: torch.randn(*s, dtype=torch.bfloat16) * 0.1)(512, 6 * 512) * 512 ** (-0.5) * 8.0)}

def reference(A_x, A_wqkv, A_wo, B_x, B_w_gate, B_w_up, B_w_down, c, w_mod):

    def _A_ref(x, wqkv, wo):
        B, S, D = x.shape
        H = 32
        Dh = D // H
        qkv = (lambda x, *d: x.permute(*d))((x @ wqkv).reshape(B, S, 3, H, Dh), 2, 0, 3, 1, 4)
        q, k, v = (qkv[0], qkv[1], qkv[2])
        scores = torch.einsum('bhqd,bhkd->bhqk', q, k) * Dh ** (-0.5)
        p = _softmax(scores, axis=-1)
        o = torch.einsum('bhqk,bhkd->bhqd', p, v)
        return (lambda x, *d: x.permute(*d))(o, 0, 2, 1, 3).reshape(B, S, D) @ wo

    def _B_ref(x, w_gate, w_up, w_down):
        g = torch.einsum('bsd,df->bsf', x, w_gate)
        u = torch.einsum('bsd,df->bsf', x, w_up)
        h = _sigmoid(g) * u
        return torch.einsum('bsf,fd->bsd', h, w_down)
    x = A_x
    mod = (_silu(c) @ w_mod).reshape(c.shape[0], 6, 1, -1)
    sa, ca, ga, sb, cb, gb = (mod[:, 0].clone(), mod[:, 1].clone(), mod[:, 2].clone(), mod[:, 3].clone(), mod[:, 4].clone(), mod[:, 5].clone())
    h = _layernorm(x, axis=-1) * (1 + ca) + sa
    x = x + ga * _A_ref(h, A_wqkv, A_wo)
    h = _layernorm(x, axis=-1) * (1 + cb) + sb
    x = x + gb * _B_ref(h, B_w_gate, B_w_up, B_w_down)
    return x
```

```cpp
#include <hip/hip_runtime.h>
#include <hip/hip_bf16.h>
#include <stdint.h>

typedef unsigned short u16;
typedef short bf16x8 __attribute__((ext_vector_type(8)));
typedef float f32x16 __attribute__((ext_vector_type(16)));
typedef float f32x4 __attribute__((ext_vector_type(4)));
typedef float f32x2 __attribute__((ext_vector_type(2)));
typedef unsigned u32x4 __attribute__((ext_vector_type(4)));
typedef unsigned u32x2 __attribute__((ext_vector_type(2)));
typedef __bf16 bf16x2_t __attribute__((ext_vector_type(2)));
typedef _Float16 f16x2_t __attribute__((ext_vector_type(2)));
#define DI __device__ __forceinline__
#define LAS __attribute__((address_space(3)))
#define MFMA(a, b, c) __builtin_amdgcn_mfma_f32_32x32x16_bf16((a), (b), (c), 0, 0, 0)

constexpr int S_ = 2048, D_ = 512, F_ = 1376, NU = 86;
constexpr float LN_EPS = 1e-5f;
constexpr float LOG2E = 1.4426950408889634f;

constexpr size_t WS_MOD = 0;
constexpr size_t WS_GS1 = 16384;
constexpr size_t WS_CSB = 32768;
constexpr size_t WS_ST2 = 65536;
constexpr size_t WS_XNF = 1u << 20;
constexpr size_t WS_WQF = WS_XNF + (2u << 20);
constexpr size_t WS_WOF = WS_WQF + 1536 * 1024;
constexpr size_t WS_WDF = WS_WOF + 512 * 1024;
constexpr size_t WS_WGU = WS_WDF + 1376 * 1024;
constexpr size_t WS_QF = WS_WGU + 2752 * 1024;
constexpr size_t WS_KF = WS_QF + (2u << 20);
constexpr size_t WS_VF = WS_KF + (2u << 20);
constexpr size_t WS_OF = WS_VF + (2u << 20);
constexpr size_t WS_X1F = WS_OF + (2u << 20);
constexpr size_t WS_HHF = WS_X1F + (2u << 20);
constexpr size_t WS_END = WS_HHF + 64 * 86 * 1024;

DI int kmap(int ks, int h, int j) { return 16 * ks + 8 * (j >> 2) + 4 * h + (j & 3); }
DI float bf2f(unsigned v) { return __uint_as_float(v << 16); }
DI float bflo(unsigned v) { return __uint_as_float(v << 16); }
DI float bfhi(unsigned v) { return __uint_as_float(v & 0xffff0000u); }
DI unsigned pkbf(float a, float b) { f32x2 v = {a, b}; return __builtin_bit_cast(unsigned, __builtin_convertvector(v, bf16x2_t)); }
DI unsigned pkh(float a, float b) { f32x2 v = {a, b}; return __builtin_bit_cast(unsigned, __builtin_convertvector(v, f16x2_t)); }
DI bf16x8 pack8(const f32x16& x, int o) {
  u32x4 p = {pkbf(x[o], x[o + 1]), pkbf(x[o + 2], x[o + 3]), pkbf(x[o + 4], x[o + 5]), pkbf(x[o + 6], x[o + 7])};
  return __builtin_bit_cast(bf16x8, p);
}
DI f32x16 zero16() { f32x16 z; _Pragma("unroll") for (int i = 0; i < 16; ++i) z[i] = 0.f; return z; }

DI void glds16(const void* gsrc, unsigned lds_dst) {
  unsigned keep;
  asm volatile("s_mov_b32 %0, m0\n\ts_mov_b32 m0, %2\n\ts_nop 0\n\tglobal_load_lds_dwordx4 %1, off\n\ts_mov_b32 m0, %0"
               : "=&s"(keep) : "v"(gsrc), "s"(lds_dst) : "memory");
}
#define VMWAIT(n) asm volatile("s_waitcnt vmcnt(" #n ")" ::: "memory")
#define RAW_BARRIER() do { asm volatile("s_waitcnt lgkmcnt(0)" ::: "memory"); __builtin_amdgcn_s_barrier(); asm volatile("" ::: "memory"); } while (0)
template <int N> DI void vmwait() { asm volatile("s_waitcnt vmcnt(%0)" :: "i"(N) : "memory"); }
DI unsigned lds_addr(const void* p) { return (unsigned)(size_t)(const LAS char*)p; }

constexpr int K0_G = 96, K0_X = 64, K0_WFR = 1536 + 512 + 1376, K0_W = K0_WFR / 16;
static_assert(K0_WFR % 16 == 0, "");

__global__ __launch_bounds__(256) void k0_prep(const u16* __restrict__ x, const u16* __restrict__ wqkv, const u16* __restrict__ wo,
                                               const u16* __restrict__ wd, const u16* __restrict__ c, const u16* __restrict__ wmod,
                                               unsigned char* __restrict__ ws) {
  __shared__ float red[4][32];
  const int tid = threadIdx.x, lane = tid & 63, wave = tid >> 6;
  int b = blockIdx.x;
  if (b < K0_G) {
    const int cq = tid & 3, rg = tid >> 2;
    u32x4 wv[8]; float sv[8];
#pragma unroll
    for (int i = 0; i < 8; ++i) wv[i] = *(const u32x4*)(wmod + (size_t)(rg + 64 * i) * 3072 + b * 32 + cq * 8);
#pragma unroll
    for (int i = 0; i < 8; ++i) { float cv = bf2f(c[rg + 64 * i]); sv[i] = cv / (1.f + __expf(-cv)); }
    float acc[8];
#pragma unroll
    for (int j = 0; j < 8; ++j) acc[j] = 0.f;
#pragma unroll
    for (int i = 0; i < 8; ++i)
#pragma unroll
      for (int q = 0; q < 4; ++q) { acc[2 * q] += sv[i] * bflo(wv[i][q]); acc[2 * q + 1] += sv[i] * bfhi(wv[i][q]); }
#pragma unroll
    for (int j = 0; j < 8; ++j) {
#pragma unroll
      for (int o = 4; o < 64; o <<= 1) acc[j] += __shfl_xor(acc[j], o);
    }
    if (lane < 4) {
#pragma unroll
      for (int j = 0; j < 8; ++j) red[wave][lane * 8 + j] = acc[j];
    }
    __syncthreads();
    if (tid < 32) {
      const float v = (red[0][tid] + red[1][tid]) + (red[2][tid] + red[3][tid]);
      const int n = b * 32 + tid;
      ((float*)(ws + WS_MOD))[n] = v;
      if (n < 1024) {
        const int k = n & 511, ks = k >> 4, kk = k & 15, h = (kk >> 2) & 1, j = (kk & 3) + 4 * (kk >> 3);
        float* gs = (float*)(ws + WS_GS1) + (ks * 2 + h) * 16;
        if (n < 512) gs[8 + j] = v; else gs[j] = 1.f + v;
      }
    }
    return;
  }
  b -= K0_G;
  if (b < K0_X) {
    const int rr = lane >> 3, cc = lane & 7, r = wave * 8 + rr, row = b * 32 + r;
    u32x4 v[8];
#pragma unroll
    for (int i = 0; i < 8; ++i) v[i] = *(const u32x4*)(x + (size_t)row * D_ + (i * 8 + cc) * 8);
    float s = 0.f;
#pragma unroll
    for (int i = 0; i < 8; ++i)
#pragma unroll
      for (int q = 0; q < 4; ++q) s += bflo(v[i][q]) + bfhi(v[i][q]);
    s += __shfl_xor(s, 1); s += __shfl_xor(s, 2); s += __shfl_xor(s, 4);
    const float mean = s * (1.f / D_);
    float s2 = 0.f;
#pragma unroll
    for (int i = 0; i < 8; ++i)
#pragma unroll
      for (int q = 0; q < 4; ++q) { float a = bflo(v[i][q]) - mean, bb = bfhi(v[i][q]) - mean; s2 += a * a + bb * bb; }
    s2 += __shfl_xor(s2, 1); s2 += __shfl_xor(s2, 2); s2 += __shfl_xor(s2, 4);
    const float rstd = 1.f / sqrtf(s2 * (1.f / D_) + LN_EPS);
    const int odd = cc & 1;
    unsigned char* xnf = ws + WS_XNF + (size_t)b * 32 * 1024;
#pragma unroll
    for (int i = 0; i < 8; ++i) {
      unsigned p[4];
#pragma unroll
      for (int q = 0; q < 4; ++q) p[q] = pkh((bflo(v[i][q]) - mean) * rstd, (bfhi(v[i][q]) - mean) * rstd);
      const unsigned s0 = odd ? p[0] : p[2], s1 = odd ? p[1] : p[3];
      const unsigned g0 = __shfl_xor(s0, 1), g1 = __shfl_xor(s1, 1);
      u32x4 o = odd ? (u32x4){g0, g1, p[2], p[3]} : (u32x4){p[0], p[1], g0, g1};
      const int ks = (i * 8 + cc) >> 1;
      *(u32x4*)(xnf + ks * 1024 + (odd * 32 + r) * 16) = o;
    }
    return;
  }
  b -= K0_X;
  {
    const int r = lane & 31, h = lane >> 5;
    const int f0 = (b * 4 + wave) * 4;
    u16 e[4][8];
    unsigned char* dst[4];
#pragma unroll
    for (int q = 0; q < 4; ++q) {
      int f = f0 + q; const u16* src; int ld, ks;
      if (f < 1536) { const int hp = f / 96, rem = f % 96; ks = rem / 3; const int t = rem % 3; src = wqkv + t * 512 + hp * 32; ld = 1536; dst[q] = ws + WS_WQF + (size_t)f * 1024; }
      else if (f < 2048) { f -= 1536; ks = f & 31; src = wo + (f >> 5) * 32; ld = 512; dst[q] = ws + WS_WOF + (size_t)f * 1024; }
      else { f -= 2048; ks = f % NU; src = wd + (f / NU) * 32; ld = 512; dst[q] = ws + WS_WDF + (size_t)f * 1024; }
#pragma unroll
      for (int j = 0; j < 8; ++j) e[q][j] = src[(size_t)kmap(ks, h, j) * ld + r];
    }
#pragma unroll
    for (int q = 0; q < 4; ++q) {
      u32x4 o;
#pragma unroll
      for (int j = 0; j < 4; ++j) o[j] = (unsigned)e[q][2 * j] | ((unsigned)e[q][2 * j + 1] << 16);
      *(u32x4*)(dst[q] + lane * 16) = o;
    }
  }
}

__global__ __launch_bounds__(256) void k0b_prep(const u16* __restrict__ wg, const u16* __restrict__ wu, unsigned char* __restrict__ ws) {
  __shared__ float red[4][32][2];
  const int tid = threadIdx.x, lane = tid & 63, wave = tid >> 6, r = lane & 31, h = lane >> 5, u = blockIdx.x;
  const float* mod = (const float*)(ws + WS_MOD);
  const u16* src = (r < 16 ? wg : wu) + u * 16 + (r & 15);
  float cs = 0.f, bs = 0.f;
#pragma unroll 2
  for (int q = 0; q < 8; ++q) {
    const int ks = wave * 8 + q;
    float w[8], g[8], sbv[8];
#pragma unroll
    for (int j = 0; j < 8; ++j) { const int k = kmap(ks, h, j); w[j] = bf2f(src[(size_t)k * F_]); g[j] = 1.f + mod[4 * 512 + k]; sbv[j] = mod[3 * 512 + k]; }
    u32x4 o;
#pragma unroll
    for (int j = 0; j < 4; ++j) {
      o[j] = pkbf(w[2 * j] * g[2 * j], w[2 * j + 1] * g[2 * j + 1]);
      cs += bflo(o[j]) + bfhi(o[j]);
      bs += sbv[2 * j] * w[2 * j] + sbv[2 * j + 1] * w[2 * j + 1];
    }
    *(u32x4*)(ws + WS_WGU + ((size_t)u * 32 + ks) * 1024 + lane * 16) = o;
  }
  cs += __shfl_xor(cs, 32); bs += __shfl_xor(bs, 32);
  if (lane < 32) { red[wave][lane][0] = cs; red[wave][lane][1] = bs; }
  __syncthreads();
  if (tid < 32) {
    const float c0 = (red[0][tid][0] + red[1][tid][0]) + (red[2][tid][0] + red[3][tid][0]);
    const float b0 = (red[0][tid][1] + red[1][tid][1]) + (red[2][tid][1] + red[3][tid][1]);
    const int hd = (tid >> 2) & 1, i = (tid & 3) + 4 * (tid >> 3);
    ((f32x2*)(ws + WS_CSB))[(u * 2 + hd) * 16 + i] = (f32x2){c0, b0};
  }
}

DI float fma_mix_lo(unsigned hv, float g, float s) { float r; asm("v_fma_mix_f32 %0, %1, %2, %3 op_sel_hi:[1,0,0]" : "=v"(r) : "v"(hv), "v"(g), "v"(s)); return r; }
DI float fma_mix_hi(unsigned hv, float g, float s) { float r; asm("v_fma_mix_f32 %0, %1, %2, %3 op_sel:[1,0,0] op_sel_hi:[1,0,0]" : "=v"(r) : "v"(hv), "v"(g), "v"(s)); return r; }
#define GLOAD(dst, voff, sbase, imm) asm volatile("global_load_dwordx4 %0, %1, %2 offset:%3" : "=v"(dst) : "v"(voff), "s"(sbase), "i"(imm) : "memory")

__global__ __launch_bounds__(256, 1) void k1_qkv(unsigned char* __restrict__ ws) {
  __shared__ __attribute__((aligned(16))) unsigned char lds[96 * 1024 + 4096];
  const int tid = threadIdx.x, lane = tid & 63, wave = __builtin_amdgcn_readfirstlane(tid >> 6), h = lane >> 5;
  const int bx = blockIdx.x, xcd = bx & 7, idx = bx >> 3;
  const int rb = (xcd >> 1) * 4 + (idx & 3), hp = (xcd & 1) * 8 + (idx >> 2);
  const int T = rb * 4 + wave;
  const unsigned lbase = lds_addr(lds);
  const unsigned char* wsrc = ws + WS_WQF + (size_t)hp * 96 * 1024 + lane * 16;
  const unsigned char* abase = ws + WS_XNF + (size_t)T * 32 * 1024;
  const unsigned voff = lane * 16;
  u32x4 a[32];
  glds16(ws + WS_GS1 + wave * 1024 + lane * 16, lbase + 96 * 1024 + wave * 1024);
#pragma unroll
  for (int c = 0; c < 8; ++c) {
#pragma unroll
    for (int i = 0; i < 3; ++i) glds16(wsrc + c * 12288 + (wave * 3 + i) * 1024, lbase + c * 12288 + (wave * 3 + i) * 1024);
    const unsigned char* ab = abase + c * 4096;
    GLOAD(a[4 * c + 0], voff, ab, 0); GLOAD(a[4 * c + 1], voff, ab, 1024); GLOAD(a[4 * c + 2], voff, ab, 2048); GLOAD(a[4 * c + 3], voff, ab, 3072);
  }
  f32x16 accq = zero16(), acck = zero16(), accv = zero16();
  const LAS unsigned char* lw = (const LAS unsigned char*)lds + lane * 16;
  const LAS unsigned char* lg = (const LAS unsigned char*)lds + 96 * 1024 + h * 64;
#pragma unroll
  for (int c = 0; c < 8; ++c) {
    switch (c) {
      case 0: vmwait<49>(); break; case 1: vmwait<42>(); break; case 2: vmwait<35>(); break; case 3: vmwait<28>(); break;
      case 4: vmwait<21>(); break; case 5: vmwait<14>(); break; case 6: vmwait<7>(); break; default: vmwait<0>(); break;
    }
    asm volatile("" : "+v"(a[4 * c]), "+v"(a[4 * c + 1]), "+v"(a[4 * c + 2]), "+v"(a[4 * c + 3]));
    RAW_BARRIER();
#pragma unroll
    for (int kk = 0; kk < 4; ++kk) {
      const int ks = 4 * c + kk;
      const f32x4 g0 = *(const LAS f32x4*)(lg + ks * 128), g1 = *(const LAS f32x4*)(lg + ks * 128 + 16);
      const f32x4 s0 = *(const LAS f32x4*)(lg + ks * 128 + 32), s1 = *(const LAS f32x4*)(lg + ks * 128 + 48);
      const bf16x8 wq = *(const LAS bf16x8*)(lw + ks * 3072), wk = *(const LAS bf16x8*)(lw + ks * 3072 + 1024), wv = *(const LAS bf16x8*)(lw + ks * 3072 + 2048);
      const u32x4 av = a[ks];
      u32x4 hp4;
      hp4[0] = pkbf(fma_mix_lo(av[0], g0[0], s0[0]), fma_mix_hi(av[0], g0[1], s0[1]));
      hp4[1] = pkbf(fma_mix_lo(av[1], g0[2], s0[2]), fma_mix_hi(av[1], g0[3], s0[3]));
      hp4[2] = pkbf(fma_mix_lo(av[2], g1[0], s1[0]), fma_mix_hi(av[2], g1[1], s1[1]));
      hp4[3] = pkbf(fma_mix_lo(av[3], g1[2], s1[2]), fma_mix_hi(av[3], g1[3], s1[3]));
      const bf16x8 hf = __builtin_bit_cast(bf16x8, hp4);
      accq = MFMA(wq, hf, accq);
      acck = MFMA(wk, hf, acck);
      accv = MFMA(hf, wv, accv);
    }
  }
  const float qs = 0.25f * LOG2E;
#pragma unroll
  for (int i = 0; i < 16; ++i) accq[i] *= qs;
  const int r = lane & 31;
#pragma unroll
  for (int e = 0; e < 2; ++e) {
    const size_t off = ((size_t)(2 * hp + e) * 64 + T) * 1024 + lane * 16;
    *(bf16x8*)(ws + WS_QF + off) = pack8(accq, 8 * e);
    *(bf16x8*)(ws + WS_KF + off) = pack8(acck, 8 * e);
    const size_t voffs = ((size_t)(2 * hp + (r >> 4)) * 64 + T) * 1024 + e * 512 + h * 256 + (r & 15) * 16;
    *(bf16x8*)(ws + WS_VF + voffs) = pack8(accv, 8 * e);
  }
}

constexpr int AT_K = 0, AT_V = 65536, AT_Q = 131072, AT_C = AT_Q + 8192, AT_X = AT_C + 4096, AT_END = AT_X + 8 * 64 * 10 * 4;
static_assert(AT_END <= 163840, "");

DI float max16(const f32x16& s) {
  float m = fmaxf(fmaxf(s[0], s[1]), fmaxf(s[2], s[3]));
  m = fmaxf(m, fmaxf(fmaxf(s[4], s[5]), fmaxf(s[6], s[7])));
  m = fmaxf(m, fmaxf(fmaxf(s[8], s[9]), fmaxf(s[10], s[11])));
  m = fmaxf(m, fmaxf(fmaxf(s[12], s[13]), fmaxf(s[14], s[15])));
  return fmaxf(m, __shfl_xor(m, 32));
}

__global__ __launch_bounds__(1024, 4) void k2_attn(unsigned char* __restrict__ ws) {
  __shared__ __attribute__((aligned(16))) unsigned char lds[AT_END];
  const int tid = threadIdx.x, lane = tid & 63, wave = __builtin_amdgcn_readfirstlane(tid >> 6), r = lane & 31, h = lane >> 5;
  const int bx = blockIdx.x, xcd = bx & 7, idx = bx >> 3;
  const int head = xcd * 4 + (idx >> 3), qb = idx & 7;
  const int qt = wave & 7, kh = wave >> 3;
  const unsigned lbase = lds_addr(lds);
  const unsigned char* kg = ws + WS_KF + (size_t)head * 65536 + lane * 16;
  const unsigned char* vg = ws + WS_VF + (size_t)head * 65536 + lane * 16;
  glds16(ws + WS_QF + ((size_t)head * 64 + qb * 8 + qt) * 1024 + lane * 16, lbase + AT_Q + qt * 1024);
  { const int t0 = kh * 32 + qt; glds16(kg + t0 * 1024, lbase + AT_K + t0 * 1024); glds16(vg + t0 * 1024, lbase + AT_V + t0 * 1024); }
#pragma unroll
  for (int i = 1; i < 4; ++i) { const int t0 = kh * 32 + i * 8 + qt; glds16(kg + t0 * 1024, lbase + AT_K + t0 * 1024); glds16(vg + t0 * 1024, lbase + AT_V + t0 * 1024); }
  { const int row = (tid >> 2) & 15; ((LAS unsigned*)((LAS unsigned char*)lds + AT_C))[tid] = (row == 0 || row == 4) ? 0x3f803f80u : 0u; }
  VMWAIT(6);
  RAW_BARRIER();
  const LAS unsigned char* L = (const LAS unsigned char*)lds;
  const bf16x8 q = *(const LAS bf16x8*)(L + AT_Q + qt * 1024 + lane * 16);
  unsigned kaddr = AT_K + kh * 32768 + lane * 16;
  unsigned vaddr = (r < 16) ? (AT_V + kh * 32768 + h * 256 + r * 16) : (AT_C + h * 256 + (r - 16) * 16);
  const unsigned vinc = (r < 16) ? 4096u : 0u;
  const unsigned kaddr0 = kaddr, vaddr0 = vaddr;

  float m;
  { const bf16x8 k0 = *(const LAS bf16x8*)(L + kaddr); m = max16(MFMA(k0, q, zero16())); }
  f32x16 cm;
#pragma unroll
  for (int i = 0; i < 16; ++i) cm[i] = -m;
  f32x16 o = zero16();
  f32x16 scur; { const bf16x8 k0 = *(const LAS bf16x8*)(L + kaddr); scur = MFMA(k0, q, cm); }
  for (int g = 0; g < 8; ++g) {
    if (g == 1) { VMWAIT(0); RAW_BARRIER(); }
#pragma unroll
    for (int tt = 0; tt < 4; ++tt) {
      const bf16x8 kn = *(const LAS bf16x8*)(L + kaddr + (tt + 1) * 1024);
      const bf16x8 v0 = *(const LAS bf16x8*)(L + vaddr + tt * 1024), v1 = *(const LAS bf16x8*)(L + vaddr + tt * 1024 + 512);
      const f32x16 snext = MFMA(kn, q, cm);
      f32x16 p;
#pragma unroll
      for (int i = 0; i < 16; ++i) p[i] = __builtin_amdgcn_exp2f(scur[i]);
      o = MFMA(v0, pack8(p, 0), o);
      o = MFMA(v1, pack8(p, 8), o);
      scur = snext;
    }
    kaddr += 4096; vaddr += vinc;
  }
  {
    float chk = fabsf(o[8]);
#pragma unroll
    for (int i = 0; i < 8; ++i) chk = fmaxf(chk, fabsf(o[i]));
    bool bad = !(chk < 1.0e30f);
#pragma unroll
    for (int i = 0; i < 9; ++i) bad = bad || (o[i] != o[i]);
    if (__any(bad)) {
      o = zero16(); kaddr = kaddr0; vaddr = vaddr0;
      for (int t = 0; t < 32; ++t) {
        const int tt = t & 3;
        const bf16x8 kc = *(const LAS bf16x8*)(L + kaddr + tt * 1024);
        const bf16x8 v0 = *(const LAS bf16x8*)(L + vaddr + tt * 1024), v1 = *(const LAS bf16x8*)(L + vaddr + tt * 1024 + 512);
        const f32x16 s = MFMA(kc, q, zero16());
        const float mn = fmaxf(m, max16(s));
        const float al = __builtin_amdgcn_exp2f(m - mn);
        m = mn;
        f32x16 p;
#pragma unroll
        for (int i = 0; i < 16; ++i) { p[i] = __builtin_amdgcn_exp2f(s[i] - mn); o[i] *= al; }
        o = MFMA(v0, pack8(p, 0), o);
        o = MFMA(v1, pack8(p, 8), o);
        if (tt == 3) { kaddr += 4096; vaddr += vinc; }
      }
    }
  }
  LAS float* X = (LAS float*)((LAS unsigned char*)lds + AT_X) + qt * 640 + lane;
  if (kh == 1) {
#pragma unroll
    for (int i = 0; i < 9; ++i) X[i * 64] = o[i];
    X[9 * 64] = m;
  }
  RAW_BARRIER();
  if (kh == 0) {
    const float m1 = X[9 * 64], mm = fmaxf(m, m1);
    const float a0 = __builtin_amdgcn_exp2f(m - mm), a1 = __builtin_amdgcn_exp2f(m1 - mm);
    const float l = o[8] * a0 + X[8 * 64] * a1, inv = 1.f / l;
    f32x16 res;
#pragma unroll
    for (int i = 0; i < 8; ++i) res[i] = (o[i] * a0 + X[i * 64] * a1) * inv;
    *(bf16x8*)(ws + WS_OF + ((size_t)(qb * 8 + qt) * 32 + head) * 1024 + lane * 16) = pack8(res, 0);
  }
}

constexpr int RS = 68;

template <int NK> DI void splitk_body(const unsigned char* abase0, const unsigned char* abase1, const unsigned char* wbase0, const unsigned char* wbase1,
                                      f32x16 (&acc)[2][2]) {
  bf16x8 a0[NK], a1[NK], w0[NK], w1[NK];
#pragma unroll
  for (int i = 0; i < NK; ++i) {
    a0[i] = *(const bf16x8*)(abase0 + i * 1024); w0[i] = *(const bf16x8*)(wbase0 + i * 1024);
    a1[i] = *(const bf16x8*)(abase1 + i * 1024); w1[i] = *(const bf16x8*)(wbase1 + i * 1024);
  }
  __builtin_amdgcn_sched_barrier(0);
#pragma unroll
  for (int i = 0; i < NK; ++i) {
    acc[0][0] = MFMA(w0[i], a0[i], acc[0][0]); acc[1][0] = MFMA(w1[i], a0[i], acc[1][0]);
    acc[0][1] = MFMA(w0[i], a1[i], acc[0][1]); acc[1][1] = MFMA(w1[i], a1[i], acc[1][1]);
  }
}
DI void splitk_reduce(LAS float* P, const f32x16 (&acc)[2][2], int wave, int lane, int tid, float (&v)[16]) {
  const int r = lane & 31, h = lane >> 5;
#pragma unroll
  for (int nt = 0; nt < 2; ++nt)
#pragma unroll
    for (int T = 0; T < 2; ++T)
#pragma unroll
      for (int qd = 0; qd < 4; ++qd) {
        f32x4 t = {acc[nt][T][4 * qd], acc[nt][T][4 * qd + 1], acc[nt][T][4 * qd + 2], acc[nt][T][4 * qd + 3]};
        *(LAS f32x4*)(P + (wave * 64 + T * 32 + r) * RS + nt * 32 + 8 * qd + 4 * h) = t;
      }
  __syncthreads();
  const int tok = tid >> 2, seg = tid & 3;
#pragma unroll
  for (int i = 0; i < 4; ++i) {
    f32x4 s = *(const LAS f32x4*)(P + tok * RS + seg * 16 + 4 * i);
#pragma unroll
    for (int w = 1; w < 4; ++w) s += *(const LAS f32x4*)(P + (w * 64 + tok) * RS + seg * 16 + 4 * i);
    v[4 * i] = s[0]; v[4 * i + 1] = s[1]; v[4 * i + 2] = s[2]; v[4 * i + 3] = s[3];
  }
}

__global__ __launch_bounds__(256, 1) void k3_wo(const u16* __restrict__ x, unsigned char* __restrict__ ws) {
  __shared__ __attribute__((aligned(16))) float P[4 * 64 * RS];
  const int tid = threadIdx.x, lane = tid & 63, wave = __builtin_amdgcn_readfirstlane(tid >> 6);
  const int bx = blockIdx.x, xcd = bx & 7, idx = bx >> 3;
  const int rb = xcd * 4 + (idx >> 3), cb = idx & 7;
  const int tok = tid >> 2, seg = tid & 3, row = rb * 64 + tok, col = cb * 64 + seg * 16;
  const u32x4 xr0 = *(const u32x4*)(x + (size_t)row * D_ + col), xr1 = *(const u32x4*)(x + (size_t)row * D_ + col + 8);
  f32x4 ga[4];
#pragma unroll
  for (int i = 0; i < 4; ++i) ga[i] = *(const f32x4*)((const float*)(ws + WS_MOD) + 2 * 512 + col + 4 * i);
  f32x16 acc[2][2] = {{zero16(), zero16()}, {zero16(), zero16()}};
  const unsigned char* ab = ws + WS_OF + ((size_t)(rb * 2) * 32 + wave * 8) * 1024 + lane * 16;
  const unsigned char* wb = ws + WS_WOF + ((size_t)(cb * 2) * 32 + wave * 8) * 1024 + lane * 16;
  splitk_body<8>(ab, ab + 32 * 1024, wb, wb + 32 * 1024, acc);
  float v[16];
  splitk_reduce((LAS float*)P, acc, wave, lane, tid, v);
  unsigned pk[8]; float xv[16];
#pragma unroll
  for (int i = 0; i < 8; ++i) {
    const unsigned xw = i < 4 ? xr0[i & 3] : xr1[i & 3];
    pk[i] = pkbf(bflo(xw) + ga[i >> 1][(2 * i) & 3] * v[2 * i], bfhi(xw) + ga[i >> 1][(2 * i + 1) & 3] * v[2 * i + 1]);
    xv[2 * i] = bflo(pk[i]); xv[2 * i + 1] = bfhi(pk[i]);
  }
  const int T = rb * 2 + (tok >> 5), ks = cb * 4 + seg;
  unsigned char* dst = ws + WS_X1F + ((size_t)T * 32 + ks) * 1024 + (tok & 31) * 16;
  *(u32x4*)dst = (u32x4){pk[0], pk[1], pk[4], pk[5]};
  *(u32x4*)(dst + 512) = (u32x4){pk[2], pk[3], pk[6], pk[7]};
  float s = 0.f;
#pragma unroll
  for (int i = 0; i < 16; ++i) s += xv[i];
  s += __shfl_xor(s, 1); s += __shfl_xor(s, 2);
  const float mean = s * (1.f / 64.f);
  float m2 = 0.f;
#pragma unroll
  for (int i = 0; i < 16; ++i) { const float d = xv[i] - mean; m2 += d * d; }
  m2 += __shfl_xor(m2, 1); m2 += __shfl_xor(m2, 2);
  if (seg == 0) ((f32x2*)(ws + WS_ST2))[row * 8 + cb] = (f32x2){mean, m2};
}

__global__ __launch_bounds__(256, 1) void k5_down(unsigned char* __restrict__ ws, u16* __restrict__ out) {
  __shared__ __attribute__((aligned(16))) float P[4 * 64 * RS];
  const int tid = threadIdx.x, lane = tid & 63, wave = __builtin_amdgcn_readfirstlane(tid >> 6);
  const int bx = blockIdx.x, xcd = bx & 7, idx = bx >> 3;
  const int rb = xcd * 4 + (idx >> 3), cb = idx & 7;
  const int tok = tid >> 2, seg = tid & 3, row = rb * 64 + tok, col = cb * 64 + seg * 16;
  const int T = rb * 2 + (tok >> 5), ksx = cb * 4 + seg;
  const unsigned char* xs = ws + WS_X1F + ((size_t)T * 32 + ksx) * 1024 + (tok & 31) * 16;
  const u32x4 xr0 = *(const u32x4*)xs, xr1 = *(const u32x4*)(xs + 512);
  f32x4 gb[4];
#pragma unroll
  for (int i = 0; i < 4; ++i) gb[i] = *(const f32x4*)((const float*)(ws + WS_MOD) + 5 * 512 + col + 4 * i);
  f32x16 acc[2][2] = {{zero16(), zero16()}, {zero16(), zero16()}};
  const int k0 = wave < 2 ? wave * 22 : 44 + (wave - 2) * 21;
  const unsigned char* ab = ws + WS_HHF + ((size_t)(rb * 2) * NU + k0) * 1024 + lane * 16;
  const unsigned char* wb = ws + WS_WDF + ((size_t)(cb * 2) * NU + k0) * 1024 + lane * 16;
  if (wave < 2) splitk_body<22>(ab, ab + NU * 1024, wb, wb + NU * 1024, acc);
  else splitk_body<21>(ab, ab + NU * 1024, wb, wb + NU * 1024, acc);
  float v[16];
  splitk_reduce((LAS float*)P, acc, wave, lane, tid, v);
  const unsigned xw[8] = {xr0[0], xr0[1], xr1[0], xr1[1], xr0[2], xr0[3], xr1[2], xr1[3]};
  unsigned pk[8];
#pragma unroll
  for (int i = 0; i < 8; ++i)
    pk[i] = pkbf(bflo(xw[i]) + gb[i >> 1][(2 * i) & 3] * v[2 * i], bfhi(xw[i]) + gb[i >> 1][(2 * i + 1) & 3] * v[2 * i + 1]);
  u16* dst = out + (size_t)row * D_ + col;
  *(u32x4*)dst = (u32x4){pk[0], pk[1], pk[2], pk[3]};
  *(u32x4*)(dst + 8) = (u32x4){pk[4], pk[5], pk[6], pk[7]};
}

constexpr int G_SLOT = 24576, G_TAB = 6 * G_SLOT;

template <int NUB> DI void k4_body(unsigned char* ws, unsigned char* lds_g, int rb, int u0, int wave, int lane) {
  const int r = lane & 31, h = lane >> 5, T = rb * 4 + wave;
  const unsigned lbase = lds_addr(lds_g);
  const unsigned voff = lane * 16;
  const unsigned char* wsrc = ws + WS_WGU + ((size_t)u0 * 32 + wave) * 1024 + lane * 16;
  const unsigned char* abase = ws + WS_X1F + (size_t)T * 32 * 1024;
  u32x4 st[4];
  u32x4 a[32];
#define K4_ISSUE(c) do { \
    _Pragma("unroll") for (int i = 0; i < NUB; ++i) glds16(wsrc + (i * 32 + (c) * 4) * 1024, lbase + ((c) % 6) * G_SLOT + (i * 4 + wave) * 1024); \
    const unsigned char* ab_ = abase + (c) * 4096; \
    GLOAD(a[4 * (c) + 0], voff, ab_, 0); GLOAD(a[4 * (c) + 1], voff, ab_, 1024); GLOAD(a[4 * (c) + 2], voff, ab_, 2048); GLOAD(a[4 * (c) + 3], voff, ab_, 3072); } while (0)
  K4_ISSUE(0); K4_ISSUE(1); K4_ISSUE(2); K4_ISSUE(3); K4_ISSUE(4); K4_ISSUE(5);
  f32x16 acc[NUB];
#pragma unroll
  for (int i = 0; i < NUB; ++i) acc[i] = zero16();
  const LAS unsigned char* lw = (const LAS unsigned char*)lds_g + lane * 16;
  constexpr int PC = NUB + 4;
#pragma unroll
  for (int c = 0; c < 8; ++c) {
    switch (c) {
      case 0: vmwait<5 * PC>(); break;
      case 1: vmwait<4 * PC>(); break;
      case 2: vmwait<4 * PC>(); break;
      case 3: vmwait<4 * PC + 5>(); break;
      case 4: vmwait<3 * PC + 5>(); break;
      case 5: vmwait<2 * PC + 5>(); break;
      case 6: vmwait<PC + 5>(); break;
      default: vmwait<0>(); break;
    }
    asm volatile("" : "+v"(a[4 * c]), "+v"(a[4 * c + 1]), "+v"(a[4 * c + 2]), "+v"(a[4 * c + 3]));
    RAW_BARRIER();
    if (c == 1) K4_ISSUE(6);
    if (c == 2) {
      K4_ISSUE(7);
      const unsigned char* sp = ws + WS_ST2 + (size_t)T * 32 * 64; const unsigned so = r * 64;
      GLOAD(st[0], so, sp, 0); GLOAD(st[1], so, sp, 16); GLOAD(st[2], so, sp, 32); GLOAD(st[3], so, sp, 48);
      glds16(ws + WS_CSB + (size_t)u0 * 256 + (wave & 1) * 1024 + lane * 16, lbase + G_TAB + (wave & 1) * 1024);
    }
#pragma unroll
    for (int kk = 0; kk < 4; ++kk) {
      const bf16x8 xf = __builtin_bit_cast(bf16x8, a[4 * c + kk]);
#pragma unroll
      for (int i = 0; i < NUB; ++i) {
        const bf16x8 wf = *(const LAS bf16x8*)(lw + (c % 6) * G_SLOT + (i * 4 + kk) * 1024);
        acc[i] = MFMA(wf, xf, acc[i]);
      }
    }
  }
#undef K4_ISSUE
  asm volatile("" : "+v"(st[0]), "+v"(st[1]), "+v"(st[2]), "+v"(st[3]));
  float mean = 0.f;
#pragma unroll
  for (int i = 0; i < 4; ++i) mean += __uint_as_float(st[i][0]) + __uint_as_float(st[i][2]);
  mean *= 0.125f;
  float m2 = 0.f;
#pragma unroll
  for (int i = 0; i < 4; ++i) {
    const float d0 = __uint_as_float(st[i][0]) - mean, d1 = __uint_as_float(st[i][2]) - mean;
    m2 += __uint_as_float(st[i][1]) + __uint_as_float(st[i][3]) + 64.f * (d0 * d0 + d1 * d1);
  }
  const float rstd = 1.f / sqrtf(m2 * (1.f / D_) + LN_EPS), nmr = -mean * rstd;
  const LAS f32x4* tab = (const LAS f32x4*)((const LAS unsigned char*)lds_g + G_TAB + h * 128);
#pragma unroll
  for (int i = 0; i < NUB; ++i) {
    float z[16];
#pragma unroll
    for (int e = 0; e < 8; ++e) {
      const f32x4 t = tab[i * 16 + e];
      z[2 * e] = acc[i][2 * e] * rstd + (nmr * t[0] + t[1]);
      z[2 * e + 1] = acc[i][2 * e + 1] * rstd + (nmr * t[2] + t[3]);
    }
    f32x16 hv;
#pragma unroll
    for (int j = 0; j < 8; ++j) hv[j] = z[8 + j] * __builtin_amdgcn_rcpf(1.f + __builtin_amdgcn_exp2f(-LOG2E * z[j]));
    *(bf16x8*)(ws + WS_HHF + ((size_t)T * NU + u0 + i) * 1024 + lane * 16) = pack8(hv, 0);
  }
}

__global__ __launch_bounds__(256, 1) void k4_glu(unsigned char* __restrict__ ws) {
  __shared__ __attribute__((aligned(16))) unsigned char lds[G_TAB + 2048];
  const int tid = threadIdx.x, lane = tid & 63, wave = __builtin_amdgcn_readfirstlane(tid >> 6);
  const int bx = blockIdx.x, xcd = bx & 7, idx = bx >> 3;
  const int rb = (xcd >> 1) * 4 + (idx & 3), ub = (xcd & 1) * 8 + (idx >> 2);
  if (ub < 6) k4_body<6>(ws, lds, rb, ub * 6, wave, lane);
  else k4_body<5>(ws, lds, rb, 36 + (ub - 6) * 5, wave, lane);
}

extern "C" void kernel_launch(void* const* d_in, const int* in_sizes, int n_in, void* d_out, int out_size, void* d_ws, size_t ws_size,
                              hipStream_t stream) {
  const u16* x = (const u16*)d_in[0];
  const u16* wqkv = (const u16*)d_in[1];
  const u16* wo = (const u16*)d_in[2];
  const u16* wg = (const u16*)d_in[4];
  const u16* wu = (const u16*)d_in[5];
  const u16* wd = (const u16*)d_in[6];
  const u16* c = (const u16*)d_in[7];
  const u16* wmod = (const u16*)d_in[8];
  unsigned char* ws = (unsigned char*)d_ws;
  k0_prep<<<K0_G + K0_X + K0_W, 256, 0, stream>>>(x, wqkv, wo, wd, c, wmod, ws);
  k0b_prep<<<NU, 256, 0, stream>>>(wg, wu, ws);
  k1_qkv<<<256, 256, 0, stream>>>(ws);
  k2_attn<<<256, 1024, 0, stream>>>(ws);
  k3_wo<<<256, 256, 0, stream>>>(x, ws);
  k4_glu<<<256, 256, 0, stream>>>(ws);
  k5_down<<<256, 256, 0, stream>>>(ws, (u16*)d_out);
}
```

```cpp
#include <hip/hip_runtime.h>
#include <hip/hip_bf16.h>
#include <stdint.h>
#include <utility>

typedef unsigned short u16;
typedef short bf16x8 __attribute__((ext_vector_type(8)));
typedef short s16x4 __attribute__((ext_vector_type(4)));
typedef float f32x16 __attribute__((ext_vector_type(16)));
typedef float f32x4 __attribute__((ext_vector_type(4)));
typedef float f32x2 __attribute__((ext_vector_type(2)));
typedef unsigned u32x4 __attribute__((ext_vector_type(4)));
typedef unsigned u32x2 __attribute__((ext_vector_type(2)));
typedef __bf16 bf16x2_t __attribute__((ext_vector_type(2)));
typedef _Float16 f16x2_t __attribute__((ext_vector_type(2)));
typedef const unsigned char* cbp;
#define DI __device__ __forceinline__
#define LAS __attribute__((address_space(3)))
#define MFMA(a, b, c) __builtin_amdgcn_mfma_f32_32x32x16_bf16((a), (b), (c), 0, 0, 0)

constexpr int F_ = 1376, NU = 86;
constexpr float LN_EPS = 1e-5f;
constexpr float LOG2E = 1.4426950408889634f;
constexpr unsigned SENT = 0xFFFFFFFFu;
constexpr int SPIN_CAP = 1 << 15;

constexpr size_t WS_EPOCH = 0;
constexpr size_t WS_MODG = 4096;
constexpr size_t WS_ST = 32768;
constexpr size_t WS_FLG = 196608;
constexpr size_t WS_KV = 1u << 20;
constexpr size_t WS_O = 5u << 20;
constexpr size_t WS_Y = 7u << 20;
constexpr size_t WS_HH = 9u << 20;

constexpr int L_WQ = 0, L_GS = 98304, L_RED = 102400, L_LNS = 102912, L_XCH = 106496;
constexpr int L_FR = 0, L_WO = 0, L_PRIV = 65536, PRIV = 11264;
constexpr int L_T2 = 155648, L_TAB = 157696, L_GBT = 158720, L_STT = 160768, L_ST3 = 161536;
constexpr int L_Y = 0, L_RA = 98304, L_WC = 65536;
constexpr int L_END = 163840;

DI float bflo(unsigned v) { return __uint_as_float(v << 16); }
DI float bfhi(unsigned v) { return __uint_as_float(v & 0xffff0000u); }
DI unsigned pkbf(float a, float b) { f32x2 v = {a, b}; return __builtin_bit_cast(unsigned, __builtin_convertvector(v, bf16x2_t)); }
DI unsigned pkh(float a, float b) { f32x2 v = {a, b}; return __builtin_bit_cast(unsigned, __builtin_convertvector(v, f16x2_t)); }
DI float rbf(float a) { return bflo(pkbf(a, a)); }
DI u32x4 pack8u(const f32x16& x, int o) {
  u32x4 p = {pkbf(x[o], x[o + 1]), pkbf(x[o + 2], x[o + 3]), pkbf(x[o + 4], x[o + 5]), pkbf(x[o + 6], x[o + 7])};
  return p;
}
DI bf16x8 asb(const u32x4& v) { return __builtin_bit_cast(bf16x8, v); }
DI u32x4 clean(u32x4 v) { v[0] = min(v[0], SENT - 1u); v[2] = min(v[2], SENT - 1u); return v; }
DI f32x16 zero16() { f32x16 z; _Pragma("unroll") for (int i = 0; i < 16; ++i) z[i] = 0.f; return z; }
DI float dot2bf(unsigned a, unsigned b, float c) { return __builtin_amdgcn_fdot2_f32_bf16(__builtin_bit_cast(bf16x2_t, a), __builtin_bit_cast(bf16x2_t, b), c, false); }
DI float fma_mix_lo(unsigned hv, float g, float s) { float r; asm("v_fma_mix_f32 %0, %1, %2, %3 op_sel_hi:[1,0,0]" : "=v"(r) : "v"(hv), "v"(g), "v"(s)); return r; }
DI float fma_mix_hi(unsigned hv, float g, float s) { float r; asm("v_fma_mix_f32 %0, %1, %2, %3 op_sel:[1,0,0] op_sel_hi:[1,0,0]" : "=v"(r) : "v"(hv), "v"(g), "v"(s)); return r; }
DI float xsum32(float s) { auto t = __builtin_amdgcn_permlane32_swap(__float_as_uint(s), __float_as_uint(s), false, false); return __uint_as_float(t[0]) + __uint_as_float(t[1]); }
DI float xmax32(float s) { auto t = __builtin_amdgcn_permlane32_swap(__float_as_uint(s), __float_as_uint(s), false, false); return fmaxf(__uint_as_float(t[0]), __uint_as_float(t[1])); }
template <int CTRL> DI float dpp_add(float v) { return v + __uint_as_float(__builtin_amdgcn_update_dpp(0u, __float_as_uint(v), CTRL, 0xf, 0xf, false)); }
DI unsigned umax3(unsigned a, unsigned b, unsigned c) { return max(max(a, b), c); }
DI bool any_lane(bool p) { return __builtin_amdgcn_ballot_w64(p) != 0ull; }

template <int N> DI void vmwait() { asm volatile("s_waitcnt vmcnt(%0)" :: "i"(N) : "memory"); }
#define LGKM0() asm volatile("s_waitcnt lgkmcnt(0)" ::: "memory")
#define BARRIER() do { asm volatile("s_waitcnt lgkmcnt(0)" ::: "memory"); __builtin_amdgcn_s_barrier(); asm volatile("" ::: "memory"); } while (0)
template <class T> DI void pin_(T& x) { asm volatile("" : "+v"(x)); }
#define PIN(x) pin_(x)
#define SLOW_B() asm volatile("; SLOW_B")
#define SLOW_E() asm volatile("; SLOW_E")
#define DEF_LD(NAME, T, INS) template <int IMM> DI void NAME(T& dst, unsigned voff, cbp sb) { asm volatile(INS : "=v"(dst) : "v"(voff), "s"(sb), "i"(IMM) : "memory"); }
#define DEF_ST(NAME, T, INS) template <int IMM> DI void NAME(unsigned voff, const T& val, cbp sb) { asm volatile(INS :: "v"(voff), "v"(val), "s"(sb), "i"(IMM) : "memory"); }
DEF_LD(gld4, u32x4, "global_load_dwordx4 %0, %1, %2 offset:%3")
DEF_LD(gld4s, u32x4, "global_load_dwordx4 %0, %1, %2 offset:%3 sc1")
DEF_LD(gld2, u32x2, "global_load_dwordx2 %0, %1, %2 offset:%3")
DEF_LD(gld2s, u32x2, "global_load_dwordx2 %0, %1, %2 offset:%3 sc1")
DEF_LD(gld1s, unsigned, "global_load_dword %0, %1, %2 offset:%3 sc1")
DEF_LD(gldu16, unsigned, "global_load_ushort %0, %1, %2 offset:%3")
DEF_ST(gst4s, u32x4, "global_store_dwordx4 %0, %1, %2 offset:%3 sc1\n\ts_nop 1")
DEF_ST(gst2s, u32x2, "global_store_dwordx2 %0, %1, %2 offset:%3 sc1")
DEF_ST(gst1s, unsigned, "global_store_dword %0, %1, %2 offset:%3 sc1")
#define GLD4(dst, voff, sb, imm) gld4<(imm)>(dst, voff, sb)
#define GLD4S(dst, voff, sb, imm) gld4s<(imm)>(dst, voff, sb)
#define GLD2(dst, voff, sb, imm) gld2<(imm)>(dst, voff, sb)
#define GLD2S(dst, voff, sb, imm) gld2s<(imm)>(dst, voff, sb)
#define GLD1S(dst, voff, sb, imm) gld1s<(imm)>(dst, voff, sb)
#define GLDU16(dst, voff, sb) gldu16<0>(dst, voff, sb)
#define GST4S(voff, val, sb, imm) gst4s<(imm)>(voff, val, sb)
#define GST2S(voff, val, sb, imm) gst2s<(imm)>(voff, val, sb)
#define GST1S(voff, val, sb, imm) gst1s<(imm)>(voff, val, sb)
template <int I0, int I1> DI void gld_v2(u32x4& v0, u32x4& v1, unsigned voff, cbp sb, unsigned long long mask) {
  asm volatile("s_mov_b64 exec, %4\n\tglobal_load_dwordx4 %0, %2, %3 offset:%5 sc1\n\tglobal_load_dwordx4 %1, %2, %3 offset:%6 sc1\n\ts_mov_b64 exec, -1"
               : "+v"(v0), "+v"(v1) : "v"(voff), "s"(sb), "s"(mask), "i"(I0), "i"(I1) : "memory");
}
template <int N> DI void vmwait3(u32x4& a, u32x4& b, u32x4& c) { asm volatile("s_waitcnt vmcnt(%3)" : "+v"(a), "+v"(b), "+v"(c) : "i"(N) : "memory"); }
DI void pin2(f32x16& a, f32x16& b) { asm volatile("" : "+v"(a), "+v"(b)); }
DI void glds_s(unsigned voff, cbp sb, unsigned ldst) {
  asm volatile("s_mov_b32 m0, %2\n\ts_nop 0\n\tglobal_load_lds_dwordx4 %0, %1" :: "v"(voff), "s"(sb), "s"(ldst) : "memory");
}
DI void glds_v(cbp va, unsigned ldst) {
  asm volatile("s_mov_b32 m0, %1\n\ts_nop 0\n\tglobal_load_lds_dwordx4 %0, off" :: "v"(va), "s"(ldst) : "memory");
}
template <int A2> DI bf16x8 trfrag(const LAS unsigned char* p) {
  typedef short v4i16_t __attribute__((ext_vector_type(4)));
  const s16x4 a = __builtin_amdgcn_ds_read_tr16_b64_v4i16((LAS v4i16_t*)p);
  const s16x4 b = __builtin_amdgcn_ds_read_tr16_b64_v4i16((LAS v4i16_t*)(p + A2));
  return __builtin_shufflevector(a, b, 0, 1, 2, 3, 4, 5, 6, 7);
}
DI float max16(const f32x16& s) {
  float m = fmaxf(fmaxf(s[0], s[1]), fmaxf(s[2], s[3]));
  m = fmaxf(m, fmaxf(fmaxf(s[4], s[5]), fmaxf(s[6], s[7])));
  m = fmaxf(m, fmaxf(fmaxf(s[8], s[9]), fmaxf(s[10], s[11])));
  m = fmaxf(m, fmaxf(fmaxf(s[12], s[13]), fmaxf(s[14], s[15])));
  return m;
}
template <int... I, class F> DI void static_for_impl(std::integer_sequence<int, I...>, F&& f) { (f(std::integral_constant<int, I>{}), ...); }
template <int N, class F> DI void static_for(F&& f) { static_for_impl(std::make_integer_sequence<int, N>{}, f); }

#define TS_DECL
#define TS(i)
#define TS_END()

constexpr int at_pre(int s) { return s == 2 || s == 5 ? 1 : 0; }
constexpr int at_l(int s) { return s + 3 <= 31 ? 3 : 0; }
constexpr int at_wg(int s) { return (s >= 1 && s <= 5) || (s >= 11 && s <= 15) || (s >= 21 && s <= 25) ? 2 : (s == 6 || s == 16) ? 1 : 0; }
constexpr int at_wg0(int s) { return s <= 6 ? 2 * (s - 1) : s <= 16 ? 11 + 2 * (s - 11) : 22 + 2 * (s - 21); }
constexpr int at_wo(int s) { return s >= 7 && s <= 10 ? 2 : 0; }
constexpr int at_post(int s) { return s < 0 ? 0 : (s == 0 ? 2 : 0) + at_wg(s) + at_wo(s); }
constexpr int at_all(int s) { return at_pre(s) + at_l(s) + at_post(s); }
constexpr int at_wait(int s) { return s == 1 ? 3 + at_post(0) + at_all(1) : at_post(s - 2) + at_all(s - 1) + at_all(s); }

DI void probe_spin(unsigned voff, cbp sb, int& budget) {
  while (budget > 0) {
    unsigned v; GLD1S(v, voff, sb, 0); vmwait<0>(); PIN(v);
    if (!any_lane(v == SENT)) break;
    __builtin_amdgcn_s_sleep(2); --budget;
  }
}

DI void probe_tag(unsigned voff, cbp sb, unsigned tag, int& budget) {
  while (budget > 0) {
    unsigned v; GLD1S(v, voff, sb, 0); vmwait<0>(); PIN(v);
    if (!any_lane(v != tag)) break;
    __builtin_amdgcn_s_sleep(2); --budget;
  }
}

__global__ __launch_bounds__(512, 2) void fused_block(const u16* __restrict__ x_, const u16* __restrict__ wqkv_, const u16* __restrict__ wo_,
                                                      const u16* __restrict__ wg_, const u16* __restrict__ wu_, const u16* __restrict__ wd_,
                                                      const u16* __restrict__ c_, const u16* __restrict__ wmod_, unsigned char* __restrict__ ws,
                                                      u16* __restrict__ out) {
  __shared__ __attribute__((aligned(1024))) unsigned char lds[L_END];
  LAS unsigned char* const L = (LAS unsigned char*)lds;
  const unsigned lb = (unsigned)(size_t)L;
  const int tid = threadIdx.x, lane = tid & 63, w = __builtin_amdgcn_readfirstlane(tid >> 6), r = lane & 31, h = lane >> 5;
  const int bx = blockIdx.x, xcd = bx & 7, idx = bx >> 3;
  const int hp = 2 * xcd + (idx & 1), rb = idx >> 1;
  const int cg = xcd * 32 + idx;
  const int tg = xcd * 4 + (idx >> 3), cb = idx & 7;
  const int p4 = xcd * 31 + idx;
  const bool act4 = idx < 31 && p4 < 242;
  const int tg4 = act4 ? p4 / 11 : 0, ug = act4 ? p4 % 11 : 0;
  const int nt4 = act4 ? min(3, 64 - 3 * tg4) : 0;
  const int unit = min(8 * ug + w, NU - 1);
  const bool hasu = act4 && 8 * ug + w < NU;
  cbp x = (cbp)x_, wqkv = (cbp)wqkv_, wo = (cbp)wo_, wd = (cbp)wd_, wsb = (cbp)ws;
  const unsigned vl16 = lane * 16;
  const u32x4 sent4 = {SENT, SENT, SENT, SENT};
  int budget = SPIN_CAP;
  TS_DECL;

  const int tq = w & 3, kh = w >> 2;
  unsigned cval, ev; u32x2 wa; u32x4 wb;
  {
    const unsigned vz = 0, vrow = tid * 6144;
    GLDU16(cval, (unsigned)(tid * 2), (cbp)c_);
    GLD2(wa, vrow, (cbp)wmod_ + cg * 8, 0);
    GLD4(wb, vrow, (cbp)wmod_ + 2048 + cg * 16, 0);
    GLD1S(ev, vz, wsb + WS_EPOCH, 0);
    cbp kvr = wsb + WS_KV + (size_t)(hp * 64 + rb * 4) * 4096 + w * 2048;
    GST4S(vl16, sent4, kvr, 0); GST4S(vl16, sent4, kvr, 1024);
    GST4S(vl16, sent4, wsb + WS_O + (size_t)((rb * 4 + (w >> 1)) * 32 + 2 * hp + (w & 1)) * 1024, 0);
  }
  u32x4 xa[16];
  {
    cbp xb = x + (size_t)(rb * 128 + tq * 32) * 1024 + kh * 512;
    const unsigned vx = r * 1024 + h * 16;
    static_for<16>([&](auto I) { GLD4(xa[I.value], vx, xb, 32 * I.value); });
    const unsigned vw = (lane >> 2) * 3072 + (lane & 3) * 16;
    cbp wqb = wqkv + hp * 64 + (size_t)w * 4 * 49152;
    static_for<12>([&](auto I) { glds_s(vw, wqb + (I.value / 3) * 49152 + (I.value % 3) * 1024, lb + L_WQ + (w * 12 + I.value) * 1024); });
  }
  TS(2);
  vmwait<31>(); PIN(cval); PIN(wa); PIN(wb); PIN(ev);
  TS(3);
  const unsigned tag = __builtin_amdgcn_readfirstlane(ev) + 1u;
  {
    const float cv = bflo(cval);
    const float sv = rbf(cv / (1.f + __expf(-cv)));
    float v[12];
    v[0] = sv * bflo(wa[0]); v[1] = sv * bfhi(wa[0]); v[2] = sv * bflo(wa[1]); v[3] = sv * bfhi(wa[1]);
#pragma unroll
    for (int q = 0; q < 4; ++q) { v[4 + 2 * q] = sv * bflo(wb[q]); v[5 + 2 * q] = sv * bfhi(wb[q]); }
    float u[6], t3[3];
#pragma unroll
    for (int p = 0; p < 6; ++p) {
      auto t = __builtin_amdgcn_permlane32_swap(__float_as_uint(v[2 * p]), __float_as_uint(v[2 * p + 1]), false, false);
      u[p] = __uint_as_float(t[0]) + __uint_as_float(t[1]);
    }
#pragma unroll
    for (int q = 0; q < 3; ++q) {
      auto t = __builtin_amdgcn_permlane16_swap(__float_as_uint(u[2 * q]), __float_as_uint(u[2 * q + 1]), false, false);
      float s = __uint_as_float(t[0]) + __uint_as_float(t[1]);
      s = dpp_add<0x128>(s); s = dpp_add<0x124>(s); s = dpp_add<0x122>(s); s = dpp_add<0x121>(s);
      t3[q] = s;
    }
    if ((lane & 15) == 0) {
      const int rho = lane >> 4, vi = (rho & 1) * 2 + (rho >> 1);
      LAS float* red = (LAS float*)(L + L_RED) + w * 12 + vi;
      red[0] = t3[0]; red[4] = t3[1]; red[8] = t3[2];
    }
    vmwait<28>();
    BARRIER();
    if (w == 0) {
      if (lane < 12) {
        const LAS float* red = (const LAS float*)(L + L_RED) + lane;
        float s = 0.f;
#pragma unroll
        for (int i = 0; i < 8; ++i) s += red[i * 12];
        const u32x2 g = {__float_as_uint(rbf(s)), tag};
        const unsigned n = lane < 4 ? 4 * cg + lane : 1024 + 8 * cg + (lane - 4);
        GST2S(n * 8, g, wsb + WS_MODG, 0);
      }
    }
  }
  TS(4);
  {
    float s0 = 0.f, s1 = 0.f, q0 = 0.f, q1 = 0.f;
    static_for<4>([&](auto G) {
      vmwait<(3 - G.value) * 4 + 12>();
      static_for<4>([&](auto I) {
        constexpr int i = 4 * G.value + I.value;
        PIN(xa[i]);
        s0 = dot2bf(xa[i][0], 0x3f803f80u, s0); q0 = dot2bf(xa[i][0], xa[i][0], q0);
        s1 = dot2bf(xa[i][1], 0x3f803f80u, s1); q1 = dot2bf(xa[i][1], xa[i][1], q1);
        s0 = dot2bf(xa[i][2], 0x3f803f80u, s0); q0 = dot2bf(xa[i][2], xa[i][2], q0);
        s1 = dot2bf(xa[i][3], 0x3f803f80u, s1); q1 = dot2bf(xa[i][3], xa[i][3], q1);
      });
    });
    const float s = xsum32(s0 + s1), q = xsum32(q0 + q1);
    if (lane < 32) *(LAS f32x2*)(L + L_LNS + (w * 32 + r) * 8) = (f32x2){s, q};
    TS(5);
    BARRIER();
    GST4S(vl16, sent4, wsb + WS_Y + (size_t)((2 * tg + (w >> 2)) * 32 + 4 * cb + (w & 3)) * 1024, 0);
    if (w == 0 && lane < 32) GST4S((unsigned)((lane >> 4) * 2048 + (lane & 15) * 16), sent4, wsb + WS_ST + (size_t)(2 * tg * 8 + cb) * 256, 0);
    if (hasu) {
      cbp hb = wsb + WS_HH + (size_t)(3 * tg4 * NU + unit) * 1024;
      for (int i = 0; i < nt4; ++i) GST4S(vl16, sent4, hb + (size_t)i * NU * 1024, 0);
    }
    u32x4 gr;
    GLD4S(gr, (unsigned)(tid * 16), wsb + WS_MODG, 0);
    const f32x2 o = *(const LAS f32x2*)(L + L_LNS + ((w ^ 4) * 32 + r) * 8);
    const float mean = (s + o[0]) * (1.f / 512.f);
    const float var = fmaxf((q + o[1]) * (1.f / 512.f) - mean * mean, 0.f);
    const float rstd = 1.f / sqrtf(var + LN_EPS), nmr = -mean * rstd;
#pragma unroll
    for (int i = 0; i < 16; ++i)
#pragma unroll
      for (int e = 0; e < 4; ++e) xa[i][e] = pkh(fmaf(bflo(xa[i][e]), rstd, nmr), fmaf(bfhi(xa[i][e]), rstd, nmr));
    static_for<16>([&](auto I) { PIN(xa[I.value]); });
    TS(6);
    vmwait<0>(); PIN(gr);
    while (any_lane(gr[1] != tag || gr[3] != tag) && budget > 0) {
      SLOW_B(); __builtin_amdgcn_s_sleep(1); --budget;
      GLD4S(gr, (unsigned)(tid * 16), wsb + WS_MODG, 0);
      vmwait<0>(); PIN(gr); SLOW_E();
    }
    {
      const int k = (2 * tid) & 511;
      f32x2 v = {__uint_as_float(gr[0]), __uint_as_float(gr[2])};
      if (tid >= 256) { v[0] = rbf(1.f + v[0]); v[1] = rbf(1.f + v[1]); }
      *(LAS f32x2*)(L + L_GS + (k >> 3) * 64 + (tid >= 256 ? 0 : 32) + (k & 7) * 4) = v;
    }
    BARRIER();
    TS(7);
    if (bx == 0 && tid == 0) GST1S(0u, tag, wsb + WS_EPOCH, 0);
  }
  u32x4 qa, qb, Kr[4], V0r[4], V1r[4];
  const int hd = w >> 2, qp = (w >> 1) & 1, kh2 = w & 1;
  {
    f32x16 accq = zero16(), acck = zero16(), accv = zero16();
    const LAS unsigned char* lw = L + L_WQ + kh * 16 * 3072 + (8 * h + ((lane & 15) >> 2)) * 64 + ((lane >> 4) & 1) * 32 + (lane & 3) * 8;
    const LAS unsigned char* lg = L + L_GS + (kh * 32 + h) * 64;
#pragma unroll
    for (int i = 0; i < 16; ++i) {
      const f32x4 g0 = *(const LAS f32x4*)(lg + i * 128), g1 = *(const LAS f32x4*)(lg + i * 128 + 16);
      const f32x4 s0 = *(const LAS f32x4*)(lg + i * 128 + 32), s1 = *(const LAS f32x4*)(lg + i * 128 + 48);
      const bf16x8 fq = trfrag<256>(lw + i * 3072), fk = trfrag<256>(lw + i * 3072 + 1024), fv = trfrag<256>(lw + i * 3072 + 2048);
      const u32x4 av = xa[i];
      u32x4 hv;
      hv[0] = pkbf(fma_mix_lo(av[0], g0[0], s0[0]), fma_mix_hi(av[0], g0[1], s0[1]));
      hv[1] = pkbf(fma_mix_lo(av[1], g0[2], s0[2]), fma_mix_hi(av[1], g0[3], s0[3]));
      hv[2] = pkbf(fma_mix_lo(av[2], g1[0], s1[0]), fma_mix_hi(av[2], g1[1], s1[1]));
      hv[3] = pkbf(fma_mix_lo(av[3], g1[2], s1[2]), fma_mix_hi(av[3], g1[3], s1[3]));
      const bf16x8 hf = asb(hv);
      acck = MFMA(fk, hf, acck);
      accv = MFMA(hf, fv, accv);
      accq = MFMA(fq, hf, accq);
    }
    TS(8);
    LAS f32x4* xo = (LAS f32x4*)(L + L_XCH + w * 6144 + lane * 16);
    const LAS f32x4* xi = (const LAS f32x4*)(L + L_XCH + (w ^ 4) * 6144 + lane * 16);
    f32x16 big; float sm[8];
    if (kh == 0) {
#pragma unroll
      for (int i = 0; i < 4; ++i) xo[i * 64] = (f32x4){accv[4 * i], accv[4 * i + 1], accv[4 * i + 2], accv[4 * i + 3]};
#pragma unroll
      for (int i = 0; i < 2; ++i) xo[(4 + i) * 64] = (f32x4){accq[8 + 4 * i], accq[9 + 4 * i], accq[10 + 4 * i], accq[11 + 4 * i]};
      big = acck;
#pragma unroll
      for (int i = 0; i < 8; ++i) sm[i] = accq[i];
    } else {
#pragma unroll
      for (int i = 0; i < 4; ++i) xo[i * 64] = (f32x4){acck[4 * i], acck[4 * i + 1], acck[4 * i + 2], acck[4 * i + 3]};
#pragma unroll
      for (int i = 0; i < 2; ++i) xo[(4 + i) * 64] = (f32x4){accq[4 * i], accq[1 + 4 * i], accq[2 + 4 * i], accq[3 + 4 * i]};
      big = accv;
#pragma unroll
      for (int i = 0; i < 8; ++i) sm[i] = accq[8 + i];
    }
    BARRIER();
#pragma unroll
    for (int i = 0; i < 4; ++i) { const f32x4 t = xi[i * 64]; big[4 * i] += t[0]; big[4 * i + 1] += t[1]; big[4 * i + 2] += t[2]; big[4 * i + 3] += t[3]; }
    const u32x4 p0 = clean(pack8u(big, 0)), p1 = clean(pack8u(big, 8));
    cbp kvo = wsb + WS_KV + (size_t)(hp * 64 + rb * 4 + tq) * 4096 + kh * 2048;
    GST4S(vl16, p0, kvo, 0); GST4S(vl16, p1, kvo, 1024);
    const float qs = 0.25f * LOG2E;
    f32x16 qf;
#pragma unroll
    for (int i = 0; i < 2; ++i) {
      const f32x4 t = xi[(4 + i) * 64];
#pragma unroll
      for (int e = 0; e < 4; ++e) qf[4 * i + e] = (sm[4 * i + e] + t[e]) * qs;
    }
    LAS unsigned char* fr = L + L_FR + tq * 6144 + lane * 16;
    *(LAS u32x4*)(fr + kh * 2048) = p0; *(LAS u32x4*)(fr + kh * 2048 + 1024) = p1;
    *(LAS u32x4*)(fr + 4096 + kh * 1024) = pack8u(qf, 0);
    BARRIER();
    TS(9);
  }
  bf16x8 WG[32];
  const unsigned lpriv = L_PRIV + w * PRIV;
  const unsigned trD = (4 * h + ((lane & 15) >> 2)) * 64 + ((lane >> 4) & 1) * 32 + (lane & 3) * 8;
  const unsigned vwo = (lane >> 2) * 1024 + (lane & 3) * 16;
  {
    const bool vdata = (r >> 4) == hd;
    const unsigned vc = ((r & 15) == 0 || (r & 15) == 4) ? 0x3f803f80u : 0u;
    const u32x4 vc4 = {vc, vc, vc, vc};
    const unsigned long long vmask = hd ? 0xFFFF0000FFFF0000ull : 0x0000FFFF0000FFFFull;
    cbp kvb = wsb + WS_KV + (size_t)hp * 262144 + kh2 * 8192 + 4096;
    const unsigned vk = vl16 + hd * 1024;
#define KV_ISSUE(S, SLOT) do { cbp pb_ = kvb + (size_t)((rb + 1 + (((S) - 2) >> 1)) & 15) * 16384; \
      GLD4S(Kr[SLOT], vk, pb_, ((S) & 1) * 4096 - 4096); \
      gld_v2<((S) & 1) * 4096 - 2048, ((S) & 1) * 4096 - 1024>(V0r[SLOT], V1r[SLOT], vl16, pb_, vmask); } while (0)
    V0r[2] = vc4; V1r[2] = vc4; V0r[3] = vc4; V1r[3] = vc4;
    KV_ISSUE(2, 2);
    {
      const LAS unsigned char* fr = L + L_FR + lane * 16;
      qa = *(const LAS u32x4*)(fr + (2 * qp) * 6144 + 4096 + hd * 1024);
      qb = *(const LAS u32x4*)(fr + (2 * qp + 1) * 6144 + 4096 + hd * 1024);
#pragma unroll
      for (int t = 0; t < 2; ++t) {
        const LAS unsigned char* ft = fr + (2 * kh2 + t) * 6144;
        Kr[t] = *(const LAS u32x4*)(ft + hd * 1024);
        const u32x4 a = *(const LAS u32x4*)(ft + 2048), b = *(const LAS u32x4*)(ft + 3072);
        V0r[t] = vdata ? a : vc4; V1r[t] = vdata ? b : vc4;
      }
    }
    BARRIER();
    float m = xmax32(fmaxf(max16(MFMA(asb(Kr[0]), asb(qa), zero16())), max16(MFMA(asb(Kr[0]), asb(qb), zero16()))));
    f32x16 cm;
#pragma unroll
    for (int i = 0; i < 16; ++i) cm[i] = -m;
    f32x16 oA = zero16(), oB = zero16();
    f32x16 sA = MFMA(asb(Kr[0]), asb(qa), cm);
    cbp wgv = ((lane & 2) ? (cbp)wu_ : (cbp)wg_) + (size_t)(lane >> 2) * (F_ * 2) + unit * 32 + (lane & 1) * 16;
    u32x4 g2a, g2b; unsigned prb;
    const unsigned vprobe = (unsigned)((lane >> 2) * 32 + (2 * kh2 + (lane & 1)) * 4 + (lane & 2) * 8);
    cbp flg = wsb + WS_FLG + hp * 512;
    TS(10);
    static_for<32>([&](auto S_) {
      constexpr int S = S_.value, c = S & 3, n = (S + 1) & 3;
      __builtin_amdgcn_sched_barrier(0);
      if constexpr (S == 2) GST1S(0u, tag, flg + rb * 32 + w * 4, 0);
      if constexpr (S == 5) GLD1S(prb, vprobe, flg, 0);
      if constexpr (S == 8) { PIN(prb); if (any_lane(prb != tag)) { SLOW_B(); probe_tag(vprobe, flg, tag, budget); SLOW_E(); } }
      if constexpr (S == 4) {
        PIN(g2a); PIN(g2b);
        while (any_lane(g2a[1] != tag || g2a[3] != tag || g2b[1] != tag || g2b[3] != tag) && budget > 0) {
          SLOW_B(); --budget;
          GLD4S(g2a, (unsigned)(tid * 32), wsb + WS_MODG + 8192, 0); GLD4S(g2b, (unsigned)(tid * 32), wsb + WS_MODG + 8192, 16);
          vmwait<0>(); PIN(g2a); PIN(g2b); SLOW_E();
        }
        const int sec = w >> 1, k = (4 * tid) & 511;
        f32x4 v = {__uint_as_float(g2a[0]), __uint_as_float(g2a[2]), __uint_as_float(g2b[0]), __uint_as_float(g2b[2])};
        if (sec == 2) { v[0] = rbf(1.f + v[0]); v[1] = rbf(1.f + v[1]); v[2] = rbf(1.f + v[2]); v[3] = rbf(1.f + v[3]); }
        if (sec == 1 || sec == 2)
          *(LAS u32x2*)(L + L_T2 + (k >> 4) * 64 + ((k >> 2) & 1) * 32 + (sec == 1 ? 16 : 0) + ((k >> 3) & 1) * 8) = (u32x2){pkbf(v[0], v[1]), pkbf(v[2], v[3])};
        if (sec != 1 && (k >> 6) == cb) *(LAS f32x4*)(L + L_TAB + (sec == 0 ? 0 : sec == 2 ? 256 : 512) + (k & 63) * 4) = v;
      }
      if constexpr (S == 10 || S == 20) {
#pragma unroll
        for (int i = 0; i < 11; ++i) WG[(S == 10 ? 0 : 11) + i] = trfrag<512>(L + lpriv + trD + i * 1024);
      }
      if constexpr (S == 11 || S == 21) LGKM0();
      if constexpr (at_l(S) != 0) KV_ISSUE(S + 3, (S + 3) & 3);
      if constexpr (S == 0) { GLD4S(g2a, (unsigned)(tid * 32), wsb + WS_MODG + 8192, 0); GLD4S(g2b, (unsigned)(tid * 32), wsb + WS_MODG + 8192, 16); }
      if constexpr (at_wg(S) != 0) {
#pragma unroll
        for (int i = 0; i < at_wg(S); ++i) { constexpr int p0 = at_wg0(S); glds_v(wgv + (size_t)(p0 + i) * (16 * F_ * 2), lb + lpriv + ((p0 + i) % 11) * 1024); }
      }
      if constexpr (at_wo(S) != 0) {
#pragma unroll
        for (int i = 0; i < 2; ++i) { const int q = 2 * (S - 7) + i; glds_s(vwo, wo + cb * 128 + (size_t)(4 * w + (q >> 1)) * 16384 + (q & 1) * 64, lb + L_WO + (8 * w + q) * 1024); }
      }
      __builtin_amdgcn_sched_barrier(0);
      f32x16 sB = MFMA(asb(Kr[c]), asb(qb), cm);
      {
        f32x16 p;
#pragma unroll
        for (int i = 0; i < 16; ++i) p[i] = __builtin_amdgcn_exp2f(sA[i]);
        oA = MFMA(asb(V0r[c]), asb(pack8u(p, 0)), oA);
        oA = MFMA(asb(V1r[c]), asb(pack8u(p, 8)), oA);
      }
      pin2(oA, sB);
      if constexpr (S < 31) {
        if constexpr (S >= 1) {
          vmwait3<at_wait(S)>(Kr[n], V0r[n], V1r[n]);
          if constexpr (S <= 9) {
            while (any_lane(umax3(umax3(Kr[n][0], Kr[n][2], V0r[n][0]), umax3(V0r[n][2], V1r[n][0], V1r[n][2]), 0u) == SENT) && budget > 0) {
              SLOW_B(); --budget; __builtin_amdgcn_s_sleep(1);
              KV_ISSUE(S + 1, n);
              vmwait3<0>(Kr[n], V0r[n], V1r[n]); SLOW_E();
            }
          }
        }
        sA = MFMA(asb(Kr[n]), asb(qa), cm);
      }
      {
        f32x16 p;
#pragma unroll
        for (int i = 0; i < 16; ++i) p[i] = __builtin_amdgcn_exp2f(sB[i]);
        oB = MFMA(asb(V0r[c]), asb(pack8u(p, 0)), oB);
        oB = MFMA(asb(V1r[c]), asb(pack8u(p, 8)), oB);
      }
      pin2(oB, sA);
      if constexpr (S < 30) TS(32 + S);
    });
    __builtin_amdgcn_sched_barrier(0);
    TS(14);
    vmwait<0>();
#pragma unroll
    for (int i = 0; i < 10; ++i) WG[22 + i] = trfrag<512>(L + lpriv + trD + i * 1024);
    float fa[9], fb[9], ma = m, mb = m;
#pragma unroll
    for (int i = 0; i < 8; ++i) { fa[i] = hd ? oA[8 + i] : oA[i]; fb[i] = hd ? oB[8 + i] : oB[i]; }
    fa[8] = hd ? oA[0] : oA[8]; fb[8] = hd ? oB[0] : oB[8];
    {
      float chk = 0.f; bool bad = !(fa[8] > 1.0e-18f) || !(fb[8] > 1.0e-18f);
#pragma unroll
      for (int i = 0; i < 9; ++i) { chk = fmaxf(chk, fmaxf(fabsf(fa[i]), fabsf(fb[i]))); bad = bad || (fa[i] != fa[i]) || (fb[i] != fb[i]); }
      bad = bad || !(chk < 1.0e30f);
      if (any_lane(bad)) {
        oA = zero16(); oB = zero16(); ma = -3.0e38f; mb = -3.0e38f;
        for (int t = 0; t < 32; ++t) {
          cbp pb = kvb + (size_t)(t >> 1) * 16384 + (t & 1) * 4096;
          u32x4 kk, a, b;
          for (;;) {
            GLD4S(kk, vk, pb, -4096); GLD4S(a, vl16, pb, -2048); GLD4S(b, vl16, pb, -1024);
            vmwait3<0>(kk, a, b);
            if (!any_lane(umax3(umax3(kk[0], kk[2], a[0]), umax3(a[2], b[0], b[2]), 0u) == SENT) || budget <= 0) break;
            --budget;
          }
          a = vdata ? a : vc4; b = vdata ? b : vc4;
          const f32x16 s1 = MFMA(asb(kk), asb(qa), zero16()), s2 = MFMA(asb(kk), asb(qb), zero16());
          const float n1 = fmaxf(ma, xmax32(max16(s1))), n2 = fmaxf(mb, xmax32(max16(s2)));
          const float a1 = __builtin_amdgcn_exp2f(ma - n1), a2 = __builtin_amdgcn_exp2f(mb - n2);
          ma = n1; mb = n2;
          f32x16 p1, p2;
#pragma unroll
          for (int i = 0; i < 16; ++i) { p1[i] = __builtin_amdgcn_exp2f(s1[i] - n1); oA[i] *= a1; p2[i] = __builtin_amdgcn_exp2f(s2[i] - n2); oB[i] *= a2; }
          oA = MFMA(asb(a), asb(pack8u(p1, 0)), oA); oA = MFMA(asb(b), asb(pack8u(p1, 8)), oA);
          oB = MFMA(asb(a), asb(pack8u(p2, 0)), oB); oB = MFMA(asb(b), asb(pack8u(p2, 8)), oB);
        }
#pragma unroll
        for (int i = 0; i < 8; ++i) { fa[i] = hd ? oA[8 + i] : oA[i]; fb[i] = hd ? oB[8 + i] : oB[i]; }
        fa[8] = hd ? oA[0] : oA[8]; fb[8] = hd ? oB[0] : oB[8];
      }
    }
    float mine[10], send[10];
#pragma unroll
    for (int i = 0; i < 9; ++i) { mine[i] = kh2 ? fb[i] : fa[i]; send[i] = kh2 ? fa[i] : fb[i]; }
    mine[9] = kh2 ? mb : ma; send[9] = kh2 ? ma : mb;
    LAS f32x4* co = (LAS f32x4*)(L + lpriv + lane * 16);
    co[0] = (f32x4){send[0], send[1], send[2], send[3]}; co[64] = (f32x4){send[4], send[5], send[6], send[7]}; co[128] = (f32x4){send[8], send[9], 0.f, 0.f};
    BARRIER();
    TS(15);
    const LAS f32x4* ci = (const LAS f32x4*)(L + L_PRIV + (w ^ 1) * PRIV + lane * 16);
    const f32x4 c0 = ci[0], c1 = ci[64], c2 = ci[128];
    const float mm = fmaxf(mine[9], c2[1]);
    const float a0 = __builtin_amdgcn_exp2f(mine[9] - mm), a1 = __builtin_amdgcn_exp2f(c2[1] - mm);
    const float inv = 1.f / (mine[8] * a0 + c2[0] * a1), b0 = a0 * inv, b1 = a1 * inv;
    f32x16 res;
#pragma unroll
    for (int i = 0; i < 4; ++i) { res[i] = mine[i] * b0 + c0[i] * b1; res[4 + i] = mine[4 + i] * b0 + c1[i] * b1; }
    GST4S(vl16, clean(pack8u(res, 0)), wsb + WS_O + (size_t)((rb * 4 + 2 * qp + kh2) * 32 + 2 * hp + hd) * 1024, 0);
#undef KV_ISSUE
  }
  TS(16);
#define ISSUE_PIECES(V, I0, NP, SB) static_for<NP>([&](auto I_) { GLD4S(V[(I0) + I_.value], vl16, (SB) + ((I_.value) >> 3) * 8192, ((I_.value) & 7) * 1024 - 4096); })
#define PIN_PIECES(V, I0, NP) static_for<NP>([&](auto I_) { PIN(V[(I0) + I_.value]); })
#define AWAIT_PIECES(V, I0, NP, SB, NYOUNG) do { \
    vmwait<NYOUNG>(); PIN_PIECES(V, I0, NP); \
    for (;;) { \
      unsigned mx_ = 0; \
      static_for<NP>([&](auto I_) { mx_ = umax3(mx_, V[(I0) + I_.value][0], V[(I0) + I_.value][2]); }); \
      if (!any_lane(mx_ == SENT) || budget <= 0) break; \
      SLOW_B(); --budget; \
      probe_spin((unsigned)(min(lane, (NP) - 1) * 1024 + 1008), (SB) - 4096, budget); \
      ISSUE_PIECES(V, I0, NP, SB); \
      vmwait<0>(); PIN_PIECES(V, I0, NP); SLOW_E(); \
    } } while (0)

  const int tt = w & 1, kq = w >> 1;
  const int T35 = 2 * tg + tt, ncol = 32 * (kq >> 1) + 16 * (kq & 1);
  float x1[8];
  f32x16 gbacc = zero16();
  const LAS unsigned char* t2p = L + L_T2 + h * 32 + (r >> 4) * 16;
  {
    u32x2 xr0, xr1; u32x4 xc;
    cbp xrow = x + (size_t)T35 * 32 * 1024;
    GLD2(xr0, (unsigned)(r * 1024 + h * 8), xrow + cb * 128 + ncol * 2, 0);
    GLD2(xr1, (unsigned)(r * 1024 + h * 8), xrow + cb * 128 + ncol * 2, 16);
    GLD4(xc, (unsigned)(r * 1024 + h * 16), xrow, 0);
    u32x4 of[8];
    cbp ob = wsb + WS_O + (size_t)(T35 * 32 + 8 * kq) * 1024 + 4096;
    ISSUE_PIECES(of, 0, 8, ob);
#pragma unroll
    for (int i = 0; i < 16; ++i) gbacc = MFMA(WG[i], *(const LAS bf16x8*)(t2p + i * 64), gbacc);
    __builtin_amdgcn_sched_barrier(0);
    TS(17);
    AWAIT_PIECES(of, 0, 8, ob, 0);
    PIN(xr0); PIN(xr1); PIN(xc);
    TS(18);
    f32x16 acc[2] = {zero16(), zero16()};
    const LAS unsigned char* wp = L + L_WO + kq * 16384 + trD;
#pragma unroll
    for (int i = 0; i < 8; ++i) {
      acc[0] = MFMA(trfrag<512>(wp + i * 2048), asb(of[i]), acc[0]);
      acc[1] = MFMA(trfrag<512>(wp + i * 2048 + 1024), asb(of[i]), acc[1]);
    }
#pragma unroll
    for (int p = 0; p < 4; ++p) {
      LAS f32x4* d = (LAS f32x4*)(L + L_PRIV + (2 * p + tt) * PRIV + 3072 + kq * 2048 + lane * 16);
      const int o = 8 * (p & 1);
      d[0] = (f32x4){acc[p >> 1][o], acc[p >> 1][o + 1], acc[p >> 1][o + 2], acc[p >> 1][o + 3]};
      d[64] = (f32x4){acc[p >> 1][o + 4], acc[p >> 1][o + 5], acc[p >> 1][o + 6], acc[p >> 1][o + 7]};
    }
    float ct = dot2bf(xc[0], 0x3f803f80u, 0.f); ct = dot2bf(xc[1], 0x3f803f80u, ct); ct = dot2bf(xc[2], 0x3f803f80u, ct); ct = dot2bf(xc[3], 0x3f803f80u, ct);
    ct = xsum32(ct) * (1.f / 16.f);
    const LAS f32x4* tab = (const LAS f32x4*)(L + L_TAB + (ncol + 4 * h) * 4);
    const f32x4 ga0 = tab[0], ga1 = tab[2], g20 = tab[16], g21 = tab[18];
    BARRIER();
    TS(19);
    const LAS f32x4* si = (const LAS f32x4*)(L + lpriv + 3072 + lane * 16);
    f32x4 v0 = (si[0] + si[128]) + (si[256] + si[384]), v1 = (si[64] + si[192]) + (si[320] + si[448]);
    const float xv[8] = {bflo(xr0[0]), bfhi(xr0[0]), bflo(xr0[1]), bfhi(xr0[1]), bflo(xr1[0]), bfhi(xr1[0]), bflo(xr1[1]), bfhi(xr1[1])};
    f32x16 y; float s1 = 0.f, s2 = 0.f;
#pragma unroll
    for (int j = 0; j < 8; ++j) {
      x1[j] = xv[j] + (j < 4 ? ga0[j & 3] * v0[j & 3] : ga1[j & 3] * v1[j & 3]);
      const float d = x1[j] - ct;
      s1 += d; s2 += d * d;
      y[j] = d * (j < 4 ? g20[j & 3] : g21[j & 3]);
    }
    GST4S(vl16, clean(pack8u(y, 0)), wsb + WS_Y + (size_t)(T35 * 32 + 4 * cb + kq) * 1024, 0);
    s1 = xsum32(s1); s2 = xsum32(s2);
    if (lane < 32) *(LAS f32x2*)(L + L_ST3 + ((tt * 4 + kq) * 32 + r) * 8) = (f32x2){s1, s2};
    BARRIER();
    if (w < 2 && lane < 32) {
      const LAS f32x2* sp = (const LAS f32x2*)(L + L_ST3 + (tt * 128 + r) * 8);
      const f32x2 t = (sp[0] + sp[32]) + (sp[64] + sp[96]);
      const u32x2 g = {min(__float_as_uint(t[0]), SENT - 1u), __float_as_uint(t[1])};
      GST2S((unsigned)(r * 8), g, wsb + WS_ST + (size_t)(T35 * 8 + cb) * 256, 0);
    }
  }
  TS(20);
  const int k0 = kq < 2 ? 22 * kq : 44 + 21 * (kq - 2);
  cbp wdb = wd + cb * 128 + tt * 64;
#define WD_ISSUE(I0, NI, LBASE, PER) static_for<NI>([&](auto I_) { \
    glds_s(vwo, wdb + (size_t)min(k0 + (I0) + I_.value, NU - 1) * 16384, lb + (LBASE) + (kq * (PER) + 2 * I_.value + tt) * 1024); })
  bf16x8 WA[14];
  {
    const int Tb = 3 * tg4;
    u32x4 ys[12]; u32x2 st[4];
    cbp yb0 = wsb + WS_Y + (size_t)(Tb * 32 + 4 * w) * 1024 + 4096;
    cbp yb1 = wsb + WS_Y + (size_t)(min(Tb + 1, 63) * 32 + 4 * w) * 1024 + 4096, yb2 = wsb + WS_Y + (size_t)(min(Tb + 2, 63) * 32 + 4 * w) * 1024 + 4096;
    cbp stb = wsb + WS_ST + (size_t)(min(Tb + min(w, 2), 63) * 8) * 256;
    const unsigned vst = h * 1024 + r * 8;
    if (act4) {
      ISSUE_PIECES(ys, 0, 4, yb0); ISSUE_PIECES(ys, 4, 4, yb1); ISSUE_PIECES(ys, 8, 4, yb2);
      GLD2S(st[0], vst, stb, 0); GLD2S(st[1], vst, stb, 256); GLD2S(st[2], vst, stb, 512); GLD2S(st[3], vst, stb, 768);
    }
    WD_ISSUE(0, 7, L_RA, 14);
    if (act4) {
#pragma unroll
      for (int i = 16; i < 32; ++i) gbacc = MFMA(WG[i], *(const LAS bf16x8*)(t2p + i * 64), gbacc);
      if ((lane & 15) == 0) {
        LAS f32x4* d = (LAS f32x4*)(L + L_GBT + w * 256 + (lane >> 4) * 64);
#pragma unroll
        for (int i = 0; i < 4; ++i) d[i] = (f32x4){gbacc[4 * i], gbacc[4 * i + 1], gbacc[4 * i + 2], gbacc[4 * i + 3]};
      }
      __builtin_amdgcn_sched_barrier(0);
      TS(21);
      LAS u32x4* yd = (LAS u32x4*)(L + L_Y + w * 4096 + lane * 16);
      AWAIT_PIECES(ys, 0, 4, yb0, 19);
#pragma unroll
      for (int i = 0; i < 4; ++i) yd[i * 64] = ys[i];
      BARRIER();
      TS(22);
      AWAIT_PIECES(ys, 4, 4, yb1, 15);
#pragma unroll
      for (int i = 0; i < 4; ++i) yd[2048 + i * 64] = ys[4 + i];
      AWAIT_PIECES(ys, 8, 4, yb2, 11);
#pragma unroll
      for (int i = 0; i < 4; ++i) yd[4096 + i * 64] = ys[8 + i];
      if (w < 3) {
        vmwait<7>(); PIN_PIECES(st, 0, 4);
        while (any_lane(max(umax3(st[0][0], st[1][0], st[2][0]), st[3][0]) == SENT) && budget > 0) {
          SLOW_B(); --budget; __builtin_amdgcn_s_sleep(1);
          GLD2S(st[0], vst, stb, 0); GLD2S(st[1], vst, stb, 256); GLD2S(st[2], vst, stb, 512); GLD2S(st[3], vst, stb, 768);
          vmwait<0>(); PIN_PIECES(st, 0, 4); SLOW_E();
        }
        const float s1 = xsum32((__uint_as_float(st[0][0]) + __uint_as_float(st[1][0])) + (__uint_as_float(st[2][0]) + __uint_as_float(st[3][0])));
        const float s2 = xsum32((__uint_as_float(st[0][1]) + __uint_as_float(st[1][1])) + (__uint_as_float(st[2][1]) + __uint_as_float(st[3][1])));
        const float dl = s1 * (1.f / 512.f), var = fmaxf(s2 * (1.f / 512.f) - dl * dl, 0.f), rstd = 1.f / sqrtf(var + LN_EPS);
        if (lane < 32) *(LAS f32x2*)(L + L_STT + (w * 32 + r) * 8) = (f32x2){rstd, -dl * rstd};
      }
      const LAS unsigned char* yl = L + L_Y + lane * 16;
      cbp hb = wsb + WS_HH + (size_t)(Tb * NU + unit) * 1024;
      auto tile_mma = [&](int t) {
        f32x16 a = zero16();
#pragma unroll
        for (int i = 0; i < 32; ++i) a = MFMA(WG[i], *(const LAS bf16x8*)(yl + t * 32768 + i * 1024), a);
        return a;
      };
      auto tile_out = [&](int t, const f32x16& a) {
        const f32x2 sr = *(const LAS f32x2*)(L + L_STT + (t * 32 + r) * 8);
        const LAS f32x4* gb = (const LAS f32x4*)(L + L_GBT + w * 256 + h * 128);
        float z[16];
#pragma unroll
        for (int i = 0; i < 4; ++i) {
          const f32x4 G = gb[i], B = gb[4 + i];
#pragma unroll
          for (int e = 0; e < 4; ++e) z[4 * i + e] = a[4 * i + e] * sr[0] + (sr[1] * G[e] + B[e]);
        }
        f32x16 hv;
#pragma unroll
        for (int j = 0; j < 8; ++j) hv[j] = z[8 + j] * __builtin_amdgcn_rcpf(1.f + __builtin_amdgcn_exp2f(-LOG2E * z[j]));
        if (hasu && t < nt4) GST4S(vl16, clean(pack8u(hv, 0)), hb + (size_t)t * NU * 1024, 0);
      };
      f32x16 a = tile_mma(0);
      TS(23);
      BARRIER();
      WD_ISSUE(7, 4, 0, 8);
      tile_out(0, a);
      TS(24);
      a = tile_mma(1);
      vmwait<4>();
      BARRIER();
      WD_ISSUE(11, 4, 32768, 8);
      tile_out(1, a);
      TS(25);
      a = tile_mma(2);
      tile_out(2, a);
      TS(26);
    } else {
      WD_ISSUE(7, 4, 0, 8); WD_ISSUE(11, 4, 32768, 8);
      vmwait<0>();
      BARRIER();
    }
#pragma unroll
    for (int i = 0; i < 14; ++i) WA[i] = trfrag<512>(L + L_RA + (kq * 14 + i) * 1024 + trD);
    vmwait<0>();
    BARRIER();
    WD_ISSUE(15, 7, L_WC, 14);
  }
  TS(27);
  {
    u32x4 hf[22];
    cbp hb = wsb + WS_HH + (size_t)(T35 * NU + k0) * 1024 + 4096;
    cbp hb2 = hb + 15 * 1024, hb3 = hb + (kq < 2 ? 21 : 20) * 1024;
    ISSUE_PIECES(hf, 0, 7, hb); ISSUE_PIECES(hf, 7, 8, hb + 7168); ISSUE_PIECES(hf, 15, 6, hb2); ISSUE_PIECES(hf, 21, 1, hb3);
    const LAS f32x4* tab = (const LAS f32x4*)(L + L_TAB + 512 + (ncol + 4 * h) * 4);
    const f32x4 gb0 = tab[0], gb1 = tab[2];
    f32x16 acc[2] = {zero16(), zero16()};
    AWAIT_PIECES(hf, 0, 7, hb, 15);
    TS(28);
#pragma unroll
    for (int i = 0; i < 7; ++i) { acc[0] = MFMA(WA[2 * i], asb(hf[i]), acc[0]); acc[1] = MFMA(WA[2 * i + 1], asb(hf[i]), acc[1]); }
    AWAIT_PIECES(hf, 7, 8, hb + 7168, 7);
    TS(29);
#pragma unroll
    for (int i = 0; i < 8; ++i) {
      const LAS unsigned char* wp = L + (i >> 2) * 32768 + (kq * 8 + 2 * (i & 3)) * 1024 + trD;
      acc[0] = MFMA(trfrag<512>(wp), asb(hf[7 + i]), acc[0]); acc[1] = MFMA(trfrag<512>(wp + 1024), asb(hf[7 + i]), acc[1]);
    }
    BARRIER();
    AWAIT_PIECES(hf, 15, 6, hb2, 1);
    AWAIT_PIECES(hf, 21, 1, hb3, 0);
    TS(30);
#pragma unroll
    for (int i = 0; i < 7; ++i) {
      if (i < 6 || kq < 2) {
        const LAS unsigned char* wp = L + L_WC + (kq * 14 + 2 * i) * 1024 + trD;
        acc[0] = MFMA(trfrag<512>(wp), asb(hf[15 + i]), acc[0]); acc[1] = MFMA(trfrag<512>(wp + 1024), asb(hf[15 + i]), acc[1]);
      }
    }
#pragma unroll
    for (int p = 0; p < 4; ++p) {
      LAS f32x4* d = (LAS f32x4*)(L + (2 * p + tt) * 8192 + kq * 2048 + lane * 16);
      const int o = 8 * (p & 1);
      d[0] = (f32x4){acc[p >> 1][o], acc[p >> 1][o + 1], acc[p >> 1][o + 2], acc[p >> 1][o + 3]};
      d[64] = (f32x4){acc[p >> 1][o + 4], acc[p >> 1][o + 5], acc[p >> 1][o + 6], acc[p >> 1][o + 7]};
    }
    BARRIER();
    TS(31);
    const LAS f32x4* si = (const LAS f32x4*)(L + w * 8192 + lane * 16);
    const f32x4 v0 = (si[0] + si[128]) + (si[256] + si[384]), v1 = (si[64] + si[192]) + (si[320] + si[448]);
    const unsigned a0 = pkbf(x1[0] + gb0[0] * v0[0], x1[1] + gb0[1] * v0[1]), a1 = pkbf(x1[2] + gb0[2] * v0[2], x1[3] + gb0[3] * v0[3]);
    const unsigned b0 = pkbf(x1[4] + gb1[0] * v1[0], x1[5] + gb1[1] * v1[1]), b1 = pkbf(x1[6] + gb1[2] * v1[2], x1[7] + gb1[3] * v1[3]);
    auto t0 = __builtin_amdgcn_permlane32_swap(a0, b0, false, false), t1 = __builtin_amdgcn_permlane32_swap(a1, b1, false, false);
    const u32x4 ov = {t0[0], t1[0], t0[1], t1[1]};
    *(u32x4*)(out + (size_t)(T35 * 32 + r) * 512 + cb * 64 + ncol + 8 * h) = ov;
  }
  TS_END();
}

extern "C" void kernel_launch(void* const* d_in, const int* in_sizes, int n_in, void* d_out, int out_size, void* d_ws, size_t ws_size,
                              hipStream_t stream) {
  fused_block<<<256, 512, 0, stream>>>((const u16*)d_in[0], (const u16*)d_in[1], (const u16*)d_in[2], (const u16*)d_in[4], (const u16*)d_in[5],
                                       (const u16*)d_in[6], (const u16*)d_in[7], (const u16*)d_in[8], (unsigned char*)d_ws, (u16*)d_out);
}
```

```cpp
#include <hip/hip_runtime.h>
#include <hip/hip_bf16.h>
#include <stdint.h>
#include <utility>

typedef unsigned short u16;
typedef short bf16x8 __attribute__((ext_vector_type(8)));
typedef short s16x4 __attribute__((ext_vector_type(4)));
typedef float f32x16 __attribute__((ext_vector_type(16)));
typedef float f32x4 __attribute__((ext_vector_type(4)));
typedef float f32x2 __attribute__((ext_vector_type(2)));
typedef unsigned u32x4 __attribute__((ext_vector_type(4)));
typedef unsigned u32x2 __attribute__((ext_vector_type(2)));
typedef __bf16 bf16x2_t __attribute__((ext_vector_type(2)));
typedef _Float16 f16x2_t __attribute__((ext_vector_type(2)));
typedef const unsigned char* cbp;
#define DI __device__ __forceinline__
#define LAS __attribute__((address_space(3)))
#define MFMA(a, b, c) __builtin_amdgcn_mfma_f32_32x32x16_bf16((a), (b), (c), 0, 0, 0)

constexpr int F_ = 1376, NU = 86;
constexpr float LN_EPS = 1e-5f;
constexpr float LOG2E = 1.4426950408889634f;
constexpr unsigned SENT = 0xFFFFFFFFu;
constexpr int SPIN_CAP = 1 << 15;

constexpr size_t WS_EPOCH = 0;
constexpr size_t WS_MK = 2048;
constexpr size_t WS_MODG = 4096;
constexpr size_t WS_ST = 32768;
constexpr size_t WS_DUMP = 16u << 20;
constexpr size_t WS_KV = 1u << 20;
constexpr size_t WS_O = 5u << 20;
constexpr size_t WS_Y = 7u << 20;
constexpr size_t WS_HH = 9u << 20;

constexpr int L_HO = 131072, L_RED = 155648, L_LNS = 156160, L_FIN = 158208;
constexpr int NX1 = 8;
constexpr int NR1 = 16, NR2 = 11, NR3 = 32 - NR1 - NR2;
constexpr int L_WO = 0, L_RB = 65536, L_RC = 0, L_PS = 40960, L_CMB = 92160;
constexpr int L_T2 = 155648, L_TAB = 157696, L_GBT = 158720, L_STT = 160768, L_ST3 = 161536;
constexpr int L_Y = 0, L_RA = 98304, L_WC = 65536;
constexpr int L_END = 163840;

DI float bflo(unsigned v) { return __uint_as_float(v << 16); }
DI float bfhi(unsigned v) { return __uint_as_float(v & 0xffff0000u); }
DI unsigned pkbf(float a, float b) { f32x2 v = {a, b}; return __builtin_bit_cast(unsigned, __builtin_convertvector(v, bf16x2_t)); }
DI float rbf(float a) { return bflo(pkbf(a, a)); }
DI u32x4 pack8u(const f32x16& x, int o) {
  u32x4 p = {pkbf(x[o], x[o + 1]), pkbf(x[o + 2], x[o + 3]), pkbf(x[o + 4], x[o + 5]), pkbf(x[o + 6], x[o + 7])};
  return p;
}
DI bf16x8 asb(const u32x4& v) { return __builtin_bit_cast(bf16x8, v); }
DI u32x4 clean(u32x4 v) { v[0] = min(v[0], SENT - 1u); v[2] = min(v[2], SENT - 1u); return v; }
DI f32x16 zero16() { f32x16 z; _Pragma("unroll") for (int i = 0; i < 16; ++i) z[i] = 0.f; return z; }
DI float dot2bf(unsigned a, unsigned b, float c) { return __builtin_amdgcn_fdot2_f32_bf16(__builtin_bit_cast(bf16x2_t, a), __builtin_bit_cast(bf16x2_t, b), c, false); }
DI float xsum32(float s) { auto t = __builtin_amdgcn_permlane32_swap(__float_as_uint(s), __float_as_uint(s), false, false); return __uint_as_float(t[0]) + __uint_as_float(t[1]); }
DI float xmax32(float s) { auto t = __builtin_amdgcn_permlane32_swap(__float_as_uint(s), __float_as_uint(s), false, false); return fmaxf(__uint_as_float(t[0]), __uint_as_float(t[1])); }
template <int CTRL> DI float dpp_add(float v) { return v + __uint_as_float(__builtin_amdgcn_update_dpp(0u, __float_as_uint(v), CTRL, 0xf, 0xf, false)); }
DI unsigned umax3(unsigned a, unsigned b, unsigned c) { return max(max(a, b), c); }
DI bool any_lane(bool p) { return __builtin_amdgcn_ballot_w64(p) != 0ull; }

template <int N> DI void vmwait() { asm volatile("s_waitcnt vmcnt(%0)" :: "i"(N) : "memory"); }
#define LGKM0() asm volatile("s_waitcnt lgkmcnt(0)" ::: "memory")
#define BARRIER() do { asm volatile("s_waitcnt lgkmcnt(0)" ::: "memory"); __builtin_amdgcn_s_barrier(); asm volatile("" ::: "memory"); } while (0)
template <class T> DI void pin_(T& x) { asm volatile("" : "+v"(x)); }
#define PIN(x) pin_(x)
#define SLOW_B() asm volatile("; SLOW_B")
#define SLOW_E() asm volatile("; SLOW_E")
#define DEF_LD(NAME, T, INS) template <int IMM> DI void NAME(T& dst, unsigned voff, cbp sb) { asm volatile(INS : "=v"(dst) : "v"(voff), "s"(sb), "i"(IMM) : "memory"); }
#define DEF_ST(NAME, T, INS) template <int IMM> DI void NAME(unsigned voff, const T& val, cbp sb) { asm volatile(INS :: "v"(voff), "v"(val), "s"(sb), "i"(IMM) : "memory"); }
DEF_LD(gld4, u32x4, "global_load_dwordx4 %0, %1, %2 offset:%3")
DEF_LD(gld4s, u32x4, "global_load_dwordx4 %0, %1, %2 offset:%3 sc1")
DEF_LD(gld2, u32x2, "global_load_dwordx2 %0, %1, %2 offset:%3")
DEF_LD(gld2s, u32x2, "global_load_dwordx2 %0, %1, %2 offset:%3 sc1")
DEF_LD(gld1s, unsigned, "global_load_dword %0, %1, %2 offset:%3 sc1")
DEF_LD(gldu16, unsigned, "global_load_ushort %0, %1, %2 offset:%3")
DEF_ST(gst4s, u32x4, "global_store_dwordx4 %0, %1, %2 offset:%3 sc1\n\ts_nop 1")
DEF_ST(gst2s, u32x2, "global_store_dwordx2 %0, %1, %2 offset:%3 sc1")
DEF_ST(gst1s, unsigned, "global_store_dword %0, %1, %2 offset:%3 sc1")
#define GLD4(dst, voff, sb, imm) gld4<(imm)>(dst, voff, sb)
#define GLD4S(dst, voff, sb, imm) gld4s<(imm)>(dst, voff, sb)
#define GLD2(dst, voff, sb, imm) gld2<(imm)>(dst, voff, sb)
#define GLD2S(dst, voff, sb, imm) gld2s<(imm)>(dst, voff, sb)
#define GLD1S(dst, voff, sb, imm) gld1s<(imm)>(dst, voff, sb)
#define GLDU16(dst, voff, sb) gldu16<0>(dst, voff, sb)
#define GST4S(voff, val, sb, imm) gst4s<(imm)>(voff, val, sb)
#define GST2S(voff, val, sb, imm) gst2s<(imm)>(voff, val, sb)
#define GST1S(voff, val, sb, imm) gst1s<(imm)>(voff, val, sb)
#define KV_LD(K, A, B) "global_load_dwordx4 " K ", %[vk], %[pb] sc1\n\ts_mov_b64 exec, %[m]\n\tglobal_load_dwordx4 " A ", %[vl], %[pb] offset:2048 sc1\n\t" \
  "global_load_dwordx4 " B ", %[vl], %[pb] offset:3072 sc1\n\ts_mov_b64 exec, -1\n\t"
#define KV_CHK(K, A, B, k0, k2, a0, a2, b0, b2) "s_waitcnt vmcnt(%[n])\n.LkvA%=:\n\tv_max3_u32 v32, " k0 ", " k2 ", " a0 "\n\tv_max3_u32 v33, " a2 ", " b0 ", " b2 "\n\t" \
  "v_max_u32 v32, v32, v33\n\tv_cmp_eq_u32 vcc, -1, v32\n\ts_cbranch_vccz .LkvB%=\n\t; SLOW_B\n\ts_cmp_lt_i32 %[bud], 1\n\ts_cbranch_scc1 .LkvB%=\n\ts_sub_i32 %[bud], %[bud], 1\n\t" \
  KV_LD(K, A, B) "s_waitcnt vmcnt(0)\n\ts_branch .LkvA%=\n\t; SLOW_E\n.LkvB%=:"
#define KV_DEF(J, K, A, B, k0, k2, a0, a2, b0, b2) \
  DI void kv_ld##J(u32x4& k, u32x4& a, u32x4& b, unsigned vk, unsigned vl, cbp pb, unsigned long long m) { \
    asm volatile(KV_LD(K, A, B) : "={" K "}"(k), "+{" A "}"(a), "+{" B "}"(b) : [vk] "v"(vk), [vl] "v"(vl), [pb] "s"(pb), [m] "s"(m) : "memory"); } \
  template <int N> DI void kv_chk##J(u32x4& k, u32x4& a, u32x4& b, unsigned vk, unsigned vl, cbp pb, unsigned long long m, int& bud) { \
    asm volatile(KV_CHK(K, A, B, k0, k2, a0, a2, b0, b2) : "+{" K "}"(k), "+{" A "}"(a), "+{" B "}"(b), [bud] "+s"(bud) \
                 : [vk] "v"(vk), [vl] "v"(vl), [pb] "s"(pb), [m] "s"(m), [n] "i"(N) : "memory", "vcc", "scc", "v32", "v33"); }
KV_DEF(0, "v[40:43]", "v[44:47]", "v[48:51]", "v40", "v42", "v44", "v46", "v48", "v50")
KV_DEF(1, "v[52:55]", "v[56:59]", "v[60:63]", "v52", "v54", "v56", "v58", "v60", "v62")
KV_DEF(2, "v[64:67]", "v[68:71]", "v[72:75]", "v64", "v66", "v68", "v70", "v72", "v74")
template <int J> DI void kv_ld(u32x4& k, u32x4& a, u32x4& b, unsigned vk, unsigned vl, cbp pb, unsigned long long m) {
  if constexpr (J == 0) kv_ld0(k, a, b, vk, vl, pb, m); else if constexpr (J == 1) kv_ld1(k, a, b, vk, vl, pb, m); else kv_ld2(k, a, b, vk, vl, pb, m);
}
template <int J, int N> DI void kv_chk(u32x4& k, u32x4& a, u32x4& b, unsigned vk, unsigned vl, cbp pb, unsigned long long m, int& bud) {
  if constexpr (J == 0) kv_chk0<N>(k, a, b, vk, vl, pb, m, bud); else if constexpr (J == 1) kv_chk1<N>(k, a, b, vk, vl, pb, m, bud); else kv_chk2<N>(k, a, b, vk, vl, pb, m, bud);
}
template <int N> DI void vmwait3(u32x4& a, u32x4& b, u32x4& c) { asm volatile("s_waitcnt vmcnt(%3)" : "+v"(a), "+v"(b), "+v"(c) : "i"(N) : "memory"); }
#define EX2(A, i, j) "v_exp_f32 v" #A #i ", v" #A #i "\n\tv_exp_f32 v" #A #j ", v" #A #j "\n\t"
#define REGION_TXT(a0,a1,a2,a3,a4,a5,a6,a7,a8,a9,a10,a11,a12,a13,a14,a15, MA, MB) \
  "v_exp_f32 " a0 ", " a0 "\n\tv_exp_f32 " a1 ", " a1 "\n\t" MA \
  "v_exp_f32 " a2 ", " a2 "\n\tv_exp_f32 " a3 ", " a3 "\n\tv_exp_f32 " a4 ", " a4 "\n\tv_exp_f32 " a5 ", " a5 "\n\t" MB \
  "v_exp_f32 " a6 ", " a6 "\n\tv_exp_f32 " a7 ", " a7 "\n\t" \
  "v_cvt_pk_bf16_f32 v32, " a0 ", " a1 "\n\tv_cvt_pk_bf16_f32 v33, " a2 ", " a3 "\n\tv_cvt_pk_bf16_f32 v34, " a4 ", " a5 "\n\tv_cvt_pk_bf16_f32 v35, " a6 ", " a7 "\n\t" \
  "v_exp_f32 " a8 ", " a8 "\n\tv_exp_f32 " a9 ", " a9 "\n\t" \
  "v_mfma_f32_32x32x16_bf16 %[oc], %[v0], v[32:35], %[oc]\n\t" \
  "v_exp_f32 " a10 ", " a10 "\n\tv_exp_f32 " a11 ", " a11 "\n\tv_exp_f32 " a12 ", " a12 "\n\tv_exp_f32 " a13 ", " a13 "\n\tv_exp_f32 " a14 ", " a14 "\n\tv_exp_f32 " a15 ", " a15 "\n\t" \
  "v_cvt_pk_bf16_f32 v36, " a8 ", " a9 "\n\tv_cvt_pk_bf16_f32 v37, " a10 ", " a11 "\n\tv_cvt_pk_bf16_f32 v38, " a12 ", " a13 "\n\tv_cvt_pk_bf16_f32 v39, " a14 ", " a15
#define REGION_EVEN(MA, MB) REGION_TXT("v0","v1","v2","v3","v4","v5","v6","v7","v8","v9","v10","v11","v12","v13","v14","v15", MA, MB)
#define REGION_ODD(MA, MB) REGION_TXT("v16","v17","v18","v19","v20","v21","v22","v23","v24","v25","v26","v27","v28","v29","v30","v31", MA, MB)
#define MA_TXT "v_mfma_f32_32x32x16_bf16 %[op], %[v1], v[36:39], %[op]\n\t"
#define MB_EVEN "v_mfma_f32_32x32x16_bf16 v[16:31], %[kn], %[qn], %[cm]\n\t"
#define MB_ODD "v_mfma_f32_32x32x16_bf16 v[0:15], %[kn], %[qn], %[cm]\n\t"
#define REGION_OPS : [s0] "+{v[0:15]}"(s0), [s1] "+{v[16:31]}"(s1), [pd] "+{v[36:39]}"(pd), [op] "+v"(op), [oc] "+v"(oc) \
                   : [v1] "v"(v1), [v0] "v"(v0), [kn] "v"(kn), [qn] "v"(qn), [cm] "v"(cm) : "v32", "v33", "v34", "v35"
template <int ODD, int HASA, int HASB>
DI void region(f32x16& s0, f32x16& s1, u32x4& pd, f32x16& op, f32x16& oc, const u32x4& v1, const u32x4& v0, const u32x4& kn, const u32x4& qn, const f32x16& cm) {
  if constexpr (!ODD && !HASA) asm volatile(REGION_EVEN("", MB_EVEN) REGION_OPS);
  else if constexpr (!ODD) asm volatile(REGION_EVEN(MA_TXT, MB_EVEN) REGION_OPS);
  else if constexpr (HASB) asm volatile(REGION_ODD(MA_TXT, MB_ODD) REGION_OPS);
  else asm volatile(REGION_ODD(MA_TXT, "") "\n\ts_nop 1\n\tv_mfma_f32_32x32x16_bf16 %[oc], %[kn], v[36:39], %[oc]\n\ts_nop 7\n\ts_nop 7" REGION_OPS);
}
DI void glds_s(unsigned voff, cbp sb, unsigned ldst) {
  asm volatile("s_mov_b32 m0, %2\n\ts_nop 0\n\tglobal_load_lds_dwordx4 %0, %1" :: "v"(voff), "s"(sb), "s"(ldst) : "memory");
}
DI void glds_v(cbp va, unsigned ldst) {
  asm volatile("s_mov_b32 m0, %1\n\ts_nop 0\n\tglobal_load_lds_dwordx4 %0, off" :: "v"(va), "s"(ldst) : "memory");
}
template <int A2> DI bf16x8 trfrag(const LAS unsigned char* p) {
  typedef short v4i16_t __attribute__((ext_vector_type(4)));
  const s16x4 a = __builtin_amdgcn_ds_read_tr16_b64_v4i16((LAS v4i16_t*)p);
  const s16x4 b = __builtin_amdgcn_ds_read_tr16_b64_v4i16((LAS v4i16_t*)(p + A2));
  return __builtin_shufflevector(a, b, 0, 1, 2, 3, 4, 5, 6, 7);
}
DI float max16(const f32x16& s) {
  float m = fmaxf(fmaxf(s[0], s[1]), fmaxf(s[2], s[3]));
  m = fmaxf(m, fmaxf(fmaxf(s[4], s[5]), fmaxf(s[6], s[7])));
  m = fmaxf(m, fmaxf(fmaxf(s[8], s[9]), fmaxf(s[10], s[11])));
  m = fmaxf(m, fmaxf(fmaxf(s[12], s[13]), fmaxf(s[14], s[15])));
  return m;
}
template <int... I, class F> DI void static_for_impl(std::integer_sequence<int, I...>, F&& f) { (f(std::integral_constant<int, I>{}), ...); }
template <int N, class F> DI void static_for(F&& f) { static_for_impl(std::make_integer_sequence<int, N>{}, f); }

#define TS_DECL
#define TS(i)
#define TS_END()

constexpr int at_l(int s) { return s + 2 <= 15 ? 3 : 0; }
constexpr int at_g(int s) { return s <= 3 ? 3 : s <= 5 ? 2 : 0; }
constexpr int at_g0(int s) { return s <= 4 ? 3 * s : 14; }
constexpr int at_w(int s) { return s <= 3 ? 3 : s <= 5 ? 5 : (s >= 9 && s <= 12) ? 4 : s == 13 ? 3 : 0; }
constexpr int at_wait(int s) { return (s == 0 ? 0 : at_w(s - 1)) + at_l(s) + at_w(s); }

__global__ __launch_bounds__(512, 2) void fused_block(const u16* __restrict__ x_, const u16* __restrict__ wqkv_, const u16* __restrict__ wo_,
                                                      const u16* __restrict__ wg_, const u16* __restrict__ wu_, const u16* __restrict__ wd_,
                                                      const u16* __restrict__ c_, const u16* __restrict__ wmod_, unsigned char* __restrict__ ws,
                                                      u16* __restrict__ out) {
  __shared__ __attribute__((aligned(1024))) unsigned char lds[L_END];
  LAS unsigned char* const L = (LAS unsigned char*)lds;
  const unsigned lb = (unsigned)(size_t)L;
  const int tid = threadIdx.x, lane = tid & 63, w = __builtin_amdgcn_readfirstlane(tid >> 6), r = lane & 31, h = lane >> 5;
  const int bx = blockIdx.x, xcd = bx & 7, idx = bx >> 3;
  const int hp = 4 * (xcd >> 1) + (idx & 3), rb = 8 * (xcd & 1) + (idx >> 2);
  const int cg = xcd * 32 + idx;
  const int tg = xcd * 4 + (idx >> 3), cb = idx & 7;
  const int p4 = xcd * 31 + idx;
  const bool act4 = idx < 31 && p4 < 242;
  const int tg4 = act4 ? p4 / 11 : 0, ug = act4 ? p4 % 11 : 0;
  const int nt4 = act4 ? min(3, 64 - 3 * tg4) : 0;
  const int unit = min(8 * ug + w, NU - 1);
  const bool hasu = act4 && 8 * ug + w < NU;
  cbp x = (cbp)x_, wqkv = (cbp)wqkv_, wo = (cbp)wo_, wd = (cbp)wd_, wsb = (cbp)ws;
  const unsigned vl16 = lane * 16;
  const u32x4 sent4 = {SENT, SENT, SENT, SENT};
  int budget = SPIN_CAP;
  TS_DECL;

  const int tq = w & 3, kh = w >> 2;
  const unsigned lx = w * 16384;
  unsigned cval, ev; u32x2 wa; u32x4 wb;
  const unsigned vrow = tid * 6144;
  cbp xb = x + (size_t)(rb * 128 + tq * 32) * 1024 + kh * 512;
  const unsigned vx0 = (h * 1024 + (r ^ h) * 16) ^ ((idx & 1) * 128);
#define X_ISSUE(I0, N) static_for<N>([&](auto I) { constexpr int i_ = (I0) + I.value; const int j_ = (i_ + 4 * idx) & 15; \
    glds_s(vx0 ^ ((2 * i_ & 15) * 16), xb + j_ * 2048, lb + lx + j_ * 1024); })
  unsigned pf0 = 0;
  const unsigned vw = (lane >> 2) * 3072 + (lane & 3) * 16;
  cbp wqb = wqkv + hp * 64 + (size_t)w * 4 * 49152;
  {
    const unsigned vz = 0;
    GLDU16(cval, (unsigned)(tid * 2), (cbp)c_);
    GLD2(wa, vrow, (cbp)wmod_ + cg * 8, 0);
    GLD1S(ev, vz, wsb + WS_EPOCH, 0);
    X_ISSUE(0, NX1);
  }
  TS(2);
  vmwait<NX1>(); PIN(cval); PIN(wa); PIN(ev);
  TS(3);
  const unsigned tag = __builtin_amdgcn_readfirstlane(ev) + 1u;
  const float sv = rbf(bflo(cval) / (1.f + __expf(-bflo(cval))));
  auto wave_sum4 = [&](float a, float b2, float c2, float d) {
    auto t = __builtin_amdgcn_permlane32_swap(__float_as_uint(a), __float_as_uint(b2), false, false);
    auto u = __builtin_amdgcn_permlane32_swap(__float_as_uint(c2), __float_as_uint(d), false, false);
    auto z = __builtin_amdgcn_permlane16_swap(__float_as_uint(__uint_as_float(t[0]) + __uint_as_float(t[1])), __float_as_uint(__uint_as_float(u[0]) + __uint_as_float(u[1])), false, false);
    float s = __uint_as_float(z[0]) + __uint_as_float(z[1]);
    s = dpp_add<0x128>(s); s = dpp_add<0x124>(s); s = dpp_add<0x122>(s); s = dpp_add<0x121>(s);
    return s;
  };
  const int vi = ((lane >> 4) & 1) * 2 + (lane >> 5);
  {
    const float s = wave_sum4(sv * bflo(wa[0]), sv * bfhi(wa[0]), sv * bflo(wa[1]), sv * bfhi(wa[1]));
    if ((lane & 15) == 0) ((LAS float*)(L + L_RED))[w * 4 + vi] = s;
  }
  BARRIER();
  TS(4);
  if (w == 0 && lane < 4) {
    const LAS float* red = (const LAS float*)(L + L_RED) + lane;
    const float t = ((red[0] + red[4]) + (red[8] + red[12])) + ((red[16] + red[20]) + (red[24] + red[28]));
    const u32x2 g = {__float_as_uint(rbf(t)), tag};
    GST2S((unsigned)((4 * cg + lane) * 8), g, wsb + WS_MODG, 0);
  }
  {
    X_ISSUE(NX1, 16 - NX1);
    if (w == 4 && lane < 32) {
      cbp pc; unsigned len;
      asm volatile("s_getpc_b64 %0\n.Lcode0:\n\ts_mov_b32 %1, .Lcode1-.Lcode0" : "=s"(pc), "=s"(len));
      const unsigned o = min((unsigned)(idx * 32 + lane) * 128u, len);
      asm volatile("global_load_dword %0, %1, %2" : "+v"(pf0) : "v"(o), "s"(pc) : "memory");
    }
    if (w >= 5 && tid < 480) {
      const int i = tid - 320, i6 = i / 6, m6 = i - 6 * i6, j = i - 96;
      cbp pa = i < 96 ? wqkv + (size_t)(16 * idx + i6) * 3072 + (m6 >> 1) * 1024 + (xcd >> 1) * 256 + (m6 & 1) * 128
                      : (cbp)wmod_ + (size_t)(16 * idx + (j >> 2)) * 6144 + 2048 + xcd * 512 + (j & 3) * 128;
      asm volatile("global_load_dword %0, %1, off" : "+v"(pf0) : "v"(pa) : "memory");
    }
    static_for<3>([&](auto I) { glds_s(vw, wqb + I.value * 1024, lb + L_HO + (3 * w + I.value) * 1024); });
    cbp kvr = wsb + WS_KV + (size_t)(hp * 64 + rb * 4) * 4096 + w * 2048;
    GST4S(vl16, sent4, kvr, 0); GST4S(vl16, sent4, kvr, 1024);
  }
  TS(52);
  const int hd = w >> 2, kq4 = w & 3;
  {
    u32x4 xa[32];
    vmwait<5>(); PIN(pf0);
    {
      const unsigned a0 = lx + r * 512 + ((h ^ (r & 15)) * 16);
#pragma unroll
      for (int i = 0; i < 16; ++i) xa[i] = *(const LAS u32x4*)(L + ((a0 ^ ((i & 7) * 32)) + (i >> 3) * 256));
      static_for<16>([&](auto I) { PIN(xa[I.value]); });
      BARRIER();
      TS(5);
#pragma unroll
      for (int i = 0; i < 16; ++i) xa[16 + i] = *(const LAS u32x4*)(L + (((a0 ^ ((i & 7) * 32)) + (i >> 3) * 256) ^ 65536));
      static_for<16>([&](auto I) { PIN(xa[16 + I.value]); });
    }
    vmwait<0>();
    BARRIER();
    TS(6);
    if (tid == 0) GST1S((unsigned)((hp * 16 + rb) * 4), tag, wsb + WS_MK, 0);
    u32x2 gsa, gca;
    const unsigned vg = (w * 64 + lane) * 8;
    GLD2S(gsa, vg, wsb + WS_MODG, 0); GLD2S(gca, vg, wsb + WS_MODG + 4096, 0);
    {
      static_for<9>([&](auto I) { constexpr int i_ = 3 + I.value; glds_s(vw, wqb + (i_ / 3) * 49152 + (i_ % 3) * 1024, lb + lx + i_ * 1024); });
    }
    LAS unsigned char* at = L + lx + 12288;
#pragma unroll
    for (int i = 0; i < 4; ++i) *(LAS u32x4*)(at + i * 1024 + lane * 16) = (u32x4){0u, 0u, 0u, 0u};
    float s0 = 0.f, s1 = 0.f, q0 = 0.f, q1 = 0.f;
#pragma unroll
    for (int i = 0; i < 16; ++i) {
      s0 = dot2bf(xa[i][0], 0x3f803f80u, s0); q0 = dot2bf(xa[i][0], xa[i][0], q0);
      s1 = dot2bf(xa[i][1], 0x3f803f80u, s1); q1 = dot2bf(xa[i][1], xa[i][1], q1);
      s0 = dot2bf(xa[i][2], 0x3f803f80u, s0); q0 = dot2bf(xa[i][2], xa[i][2], q0);
      s1 = dot2bf(xa[i][3], 0x3f803f80u, s1); q1 = dot2bf(xa[i][3], xa[i][3], q1);
    }
    const float s = xsum32(s0 + s1), q = xsum32(q0 + q1);
    if (lane < 32) *(LAS f32x2*)(L + L_LNS + (w * 32 + r) * 8) = (f32x2){s, q};
    TS(7);
    {
      vmwait<0>(); PIN(gsa); PIN(gca);
      bf16x8 wr[12];
      const LAS unsigned char* lw = L + (8 * h + ((lane & 15) >> 2)) * 64 + ((lane >> 4) & 1) * 32 + (lane & 3) * 8;
#pragma unroll
      for (int i = 0; i < 12; ++i) wr[i] = trfrag<256>(lw + (i < 3 ? L_HO + w * 3072 : lx) + i * 1024);
      static_for<12>([&](auto I) { PIN(wr[I.value]); });
      TS(8);
      while (any_lane(gsa[1] != tag || gca[1] != tag) && budget > 0) {
        SLOW_B(); --budget;
        GLD2S(gsa, vg, wsb + WS_MODG, 0); GLD2S(gca, vg, wsb + WS_MODG + 4096, 0);
        vmwait<0>(); PIN(gsa); PIN(gca); SLOW_E();
      }
      TS(9);
      {
        const int kk = lane & 15, rho = (kk & 3) | ((kk & 4) << 1) | ((kk & 8) >> 1);
        LAS u16* ap = (LAS u16*)(at + (lane >> 4) * 1024 + (kk >> 3) * 512 + (kk & 7) * 2);
        const unsigned p = pkbf(1.f + __uint_as_float(gca[0]), __uint_as_float(gsa[0]));
        ap[rho * 8] = (u16)p; ap[128] = (u16)p; ap[136] = (u16)(p >> 16);
      }
      f32x2 gb[3] = {{0.f, 0.f}, {0.f, 0.f}, {0.f, 0.f}};
      bf16x8 af[4];
      af[0] = *(const LAS bf16x8*)(at + lane * 16); af[1] = *(const LAS bf16x8*)(at + 1024 + lane * 16);
      f32x16 dp = MFMA(af[0], wr[0], zero16());
      static_for<12>([&](auto I) {
        constexpr int n = I.value;
        f32x16 d;
        if constexpr (n == 2 || n == 5) af[n / 3 + 2] = *(const LAS bf16x8*)(at + (n / 3 + 2) * 1024 + lane * 16);
        if constexpr (n < 11) d = MFMA(af[(n + 1) / 3], wr[n + 1], zero16());
        __builtin_amdgcn_sched_barrier(0);
        *(LAS u32x4*)(L + lx + n * 1024 + lane * 16) = pack8u(dp, 0);
        gb[n % 3][0] += dp[8]; gb[n % 3][1] += dp[9]; PIN(gb[n % 3]);
        __builtin_amdgcn_sched_barrier(0);
        if constexpr (n < 11) dp = d;
      });
      if (lane < 32) {
#pragma unroll
        for (int j = 0; j < 3; ++j) *(LAS f32x2*)(at + j * 256 + r * 8) = gb[j];
      }
    }
    BARRIER();
    TS(10);
    if (bx == 0 && tid == 0) GST1S(0u, tag, wsb + WS_EPOCH, 0);
    GLD4(wb, vrow, (cbp)wmod_ + 2048 + cg * 16, 0);
    unsigned mk;
    const unsigned vmk = (hp * 16 + (lane & 15)) * 4;
    GLD1S(mk, vmk, wsb + WS_MK, 0);
    const float qs = 0.25f * LOG2E;
    const f32x2 o = *(const LAS f32x2*)(L + L_LNS + ((w ^ 4) * 32 + r) * 8);
    const float mean = (s + o[0]) * (1.f / 512.f);
    const float var = fmaxf((q + o[1]) * (1.f / 512.f) - mean * mean, 0.f);
    const float rstd = 1.f / sqrtf(var + LN_EPS), nmr = -mean * rstd;
    f32x2 cst;
    {
      const LAS unsigned char* pp = L + 12288 + (kh ? 2 : h) * 256 + r * 8;
      f32x2 t[8];
#pragma unroll
      for (int i = 0; i < 8; ++i) t[i] = *(const LAS f32x2*)(pp + i * 16384);
      cst = ((t[0] + t[1]) + (t[2] + t[3])) + ((t[4] + t[5]) + (t[6] + t[7]));
    }
    LAS float* fin = (LAS float*)(L + L_FIN + w * 512);
    const LAS unsigned char* lf0 = L + kh * 65536 + lane * 16;
    const LAS unsigned char* lf1 = L + (kh ^ 1) * 65536 + lane * 16;
    LAS unsigned char* fr = L + L_HO + tq * 6144 + lane * 16;
    cbp kvo = wsb + WS_KV + (size_t)(hp * 64 + rb * 4 + tq) * 4096 + kh * 2048;
    f32x16 big = zero16();
    if (kh == 0) {
      const float sc = h ? 1.f : qs;
      fin[h * 64 + r] = cst[0] * sc; fin[h * 64 + 32 + r] = cst[1] * sc;
      f32x16 accq = zero16(), ck, cq;
      bf16x8 fk[3], fq[3];
      static_for<34>([&](auto I) {
        constexpr int i = I.value, j = i - 2;
        if constexpr (i < 32) {
          const LAS unsigned char* p = (i < 16 ? lf0 : lf1) + ((i >> 2) & 3) * 16384 + (i & 3) * 3072;
          fk[i % 3] = *(const LAS bf16x8*)(p + 1024); fq[i % 3] = *(const LAS bf16x8*)p;
        }
        if constexpr (i >= 24 && i < 28) {
          constexpr int n = i - 24;
          const LAS f32x4* cp = (const LAS f32x4*)(fin + 4 * h) + 2 * n;
          const f32x4 G = cp[16], B = cp[24], Gq = cp[0], Bq = cp[8];
#pragma unroll
          for (int e = 0; e < 4; ++e) { ck[4 * n + e] = fmaf(nmr, G[e], B[e]); cq[4 * n + e] = fmaf(nmr, Gq[e], Bq[e]); }
        }
        __builtin_amdgcn_sched_barrier(0);
        if constexpr (j >= 0) {
          big = MFMA(fk[j % 3], asb(xa[j]), big);
          accq = MFMA(fq[j % 3], asb(xa[j]), accq);
        }
        __builtin_amdgcn_sched_barrier(0);
      });
      TS(11);
      const float rq = rstd * qs;
#pragma unroll
      for (int i = 0; i < 16; ++i) { big[i] = fmaf(big[i], rstd, ck[i]); accq[i] = fmaf(accq[i], rq, cq[i]); }
      *(LAS u32x4*)(fr + 4096) = pack8u(accq, 0); *(LAS u32x4*)(fr + 5120) = pack8u(accq, 8);
    } else {
      if (lane < 32) { fin[r] = rstd; fin[32 + r] = nmr; }
      f32x16 cr, cv;
      bf16x8 fv[4];
      static_for<35>([&](auto I) {
        constexpr int i = I.value, j = i - 3;
        if constexpr (i < 32) fv[i & 3] = *(const LAS bf16x8*)((i < 16 ? lf0 : lf1) + ((i >> 2) & 3) * 16384 + (i & 3) * 3072 + 2048);
        if constexpr (i >= 24 && i < 28) {
          constexpr int n = i - 24;
          const LAS f32x4* cp = (const LAS f32x4*)(fin + 4 * h) + 2 * n;
          const f32x4 R = cp[0], M = cp[8];
#pragma unroll
          for (int e = 0; e < 4; ++e) { cr[4 * n + e] = R[e]; cv[4 * n + e] = fmaf(M[e], cst[0], cst[1]); }
        }
        __builtin_amdgcn_sched_barrier(0);
        if constexpr (j >= 0) big = MFMA(asb(xa[j]), fv[j & 3], big);
        __builtin_amdgcn_sched_barrier(0);
      });
      TS(11);
#pragma unroll
      for (int i = 0; i < 16; ++i) big[i] = fmaf(big[i], cr[i], cv[i]);
    }
    const u32x4 p0 = clean(pack8u(big, 0)), p1 = clean(pack8u(big, 8));
    TS(48);
    vmwait<0>(); PIN(wb); PIN(mk);
    TS(49);
    GST4S(vl16, p0, kvo, 0); GST4S(vl16, p1, kvo, 1024);
    {
      const float t1 = wave_sum4(sv * bflo(wb[0]), sv * bfhi(wb[0]), sv * bflo(wb[1]), sv * bfhi(wb[1]));
      const float t2 = wave_sum4(sv * bflo(wb[2]), sv * bfhi(wb[2]), sv * bflo(wb[3]), sv * bfhi(wb[3]));
      if ((lane & 15) == 0) { LAS float* red = (LAS float*)(L + L_RED + 128) + w * 8 + vi; red[0] = t1; red[4] = t2; }
    }
    while (any_lane(mk != tag) && budget > 0) {
      SLOW_B(); --budget;
      GLD1S(mk, vmk, wsb + WS_MK, 0);
      vmwait<0>(); PIN(mk); SLOW_E();
    }
    TS(50);
    *(LAS u32x4*)(fr + kh * 2048) = p0; *(LAS u32x4*)(fr + kh * 2048 + 1024) = p1;
  }
  bf16x8 WO[16];
  u32x2 xr0, xr1; u32x4 xc;
  bf16x8 WG[32];
  const unsigned vwo = (lane >> 2) * 1024 + (lane & 3) * 16;
  cbp wgv;
  unsigned trG;
  {
    const int mat = (lane >> 4) & 1, row = 2 * w + h, su = ((lane & 15) >> 1) ^ ((row & 3) + 4 * mat);
    wgv = (mat ? (cbp)wu_ : (cbp)wg_) + (size_t)row * (F_ * 2) + min(8 * ug + su, NU - 1) * 32 + (lane & 1) * 16;
    const int q = (lane & 15) >> 2;
    trG = (4 * h + q) * 512 + mat * 256 + (w ^ (q + 4 * mat)) * 32 + (lane & 3) * 8;
  }
  const int rot3 = idx / 11, rot4 = idx >> 3;
#define WG_ISSUE(K0, N, I, LB) do { int i_ = (I) + rot3 * (((N) + 1) / 3); i_ -= i_ >= (N) ? (N) : 0; \
    glds_v(wgv + (size_t)((K0) + i_) * (16 * F_ * 2), lb + (LB) + i_ * 8192 + w * 1024); } while (0)
#define WG_READ(KS, LSRC) WG[KS] = trfrag<4096>(L + (LSRC) + trG)
  {
    u32x4 qf[4], Kr[3], V0r[3], V1r[3];
    const bool vdata = (r >> 4) == hd;
    const unsigned vc = ((r & 15) == 0 || (r & 15) == 4) ? 0x3f803f80u : 0u;
    const u32x4 vc4 = {vc, vc, vc, vc};
    const unsigned long long vmask = hd ? 0xFFFF0000FFFF0000ull : 0x0000FFFF0000FFFFull;
    cbp kvb = wsb + WS_KV + (size_t)hp * 262144 + kq4 * 4096;
    const unsigned vk = vl16 + hd * 1024;
#define KV_ISSUE(S) kv_ld<(S) % 3>(Kr[(S) % 3], V0r[(S) % 3], V1r[(S) % 3], vk, vl16, kvb + (size_t)((rb + (S)) & 15) * 16384, vmask)
    V0r[1] = vc4; V1r[1] = vc4; V0r[2] = vc4; V1r[2] = vc4;
    BARRIER();
    TS(12);
    {
      const LAS unsigned char* fr = L + L_HO + lane * 16;
#pragma unroll
      for (int t = 0; t < 4; ++t) qf[t] = *(const LAS u32x4*)(fr + t * 6144 + 4096 + hd * 1024);
      const LAS unsigned char* ft = fr + kq4 * 6144;
      Kr[0] = *(const LAS u32x4*)(ft + hd * 1024);
      const u32x4 a = *(const LAS u32x4*)(ft + 2048), b = *(const LAS u32x4*)(ft + 3072);
      V0r[0] = vdata ? a : vc4; V1r[0] = vdata ? b : vc4;
    }
    float m = xmax32(fmaxf(fmaxf(max16(MFMA(asb(Kr[0]), asb(qf[0]), zero16())), max16(MFMA(asb(Kr[0]), asb(qf[1]), zero16()))),
                           fmaxf(max16(MFMA(asb(Kr[0]), asb(qf[2]), zero16())), max16(MFMA(asb(Kr[0]), asb(qf[3]), zero16())))));
    f32x16 cm;
#pragma unroll
    for (int i = 0; i < 16; ++i) cm[i] = -m;
    f32x16 o[4] = {zero16(), zero16(), zero16(), zero16()};
    f32x16 s0 = MFMA(asb(Kr[0]), asb(qf[0]), cm), s1 = zero16();
    u32x4 pd = {0u, 0u, 0u, 0u};
    u32x4 g2a, g2b; int bud2 = SPIN_CAP;
    TS(51);
    KV_ISSUE(1);
    asm volatile("s_nop 7\n\ts_nop 7" : "+v"(s0));
    TS(13);
    static_for<16>([&](auto S_) {
      constexpr int S = S_.value, c = S % 3, n = (S + 1) % 3, p = (S + 2) % 3;
      if constexpr (S == 8 || S == 9) BARRIER();
      if constexpr (S == 8) {
        if (w == 5 && lane < 8) {
          const LAS float* red = (const LAS float*)(L + L_RED + 128) + lane;
          const float t = ((red[0] + red[8]) + (red[16] + red[24])) + ((red[32] + red[40]) + (red[48] + red[56]));
          const u32x2 g = {__float_as_uint(rbf(t)), tag};
          GST2S((unsigned)((1024 + 8 * cg + lane) * 8), g, wsb + WS_MODG, 0);
        }
        static_for<4>([&](auto I) { WG_READ(I.value, I.value * 8192); });
      }
      region<0, S != 0, 1>(s0, s1, pd, o[3], o[0], V1r[p], V0r[c], Kr[c], qf[1], cm);
      if constexpr (at_l(S) != 0) KV_ISSUE(S + 2);
      if constexpr (S == 8) static_for<4>([&](auto I) { WG_READ(4 + I.value, (4 + I.value) * 8192); });
      region<1, 1, 1>(s0, s1, pd, o[0], o[1], V1r[c], V0r[c], Kr[c], qf[2], cm);
      if constexpr (S <= 5) static_for<at_g(S)>([&](auto I) { WG_ISSUE(0, NR1, at_g0(S) + I.value, 0); });
      if constexpr (S == 4) {
        const u32x4 m4 = {SENT, SENT, SENT, SENT};
        GST4S(vl16, m4, wsb + WS_O + (size_t)((rb * 4 + (w >> 1)) * 32 + 2 * hp + (w & 1)) * 1024, 0);
        GST4S(vl16, m4, wsb + WS_Y + (size_t)((2 * tg + (w >> 2)) * 32 + 4 * cb + (w & 3)) * 1024, 0);
        if (lane < 4) GST4S(vl16, m4, wsb + WS_ST + (size_t)(2 * tg * 8 + cb) * 256 + (w >> 2) * 2048 + (w & 3) * 64, 0);
      }
      if constexpr (S == 5) {
        const u32x4 m4 = {SENT, SENT, SENT, SENT};
        static_for<3>([&](auto I) {
          GST4S(vl16, m4, hasu && I.value < nt4 ? wsb + WS_HH + (size_t)((3 * tg4 + I.value) * NU + unit) * 1024 : wsb + WS_DUMP + (size_t)(bx * 8 + w) * 1024, 0); });
      }
      if constexpr (S == 9 || S == 10) static_for<4>([&](auto I) {
        const int q = (4 * (S - 9) + I.value + 2 * rot4) & 7;
        glds_s(vwo, wo + cb * 128 + (size_t)(4 * w + (q >> 1)) * 16384 + (q & 1) * 64, lb + L_WO + (8 * w + q) * 1024); });
      if constexpr (S >= 11 && S <= 13) static_for<(S == 13 ? 3 : 4)>([&](auto I) { WG_ISSUE(NR1, NR2, 4 * (S - 11) + I.value, L_RB); });
      if constexpr (S == 8) static_for<4>([&](auto I) { WG_READ(8 + I.value, (8 + I.value) * 8192); });
      region<0, 1, 1>(s0, s1, pd, o[1], o[2], V1r[c], V0r[c], Kr[c], qf[3], cm);
      if constexpr (S < 15) {
        kv_chk<n, at_wait(S)>(Kr[n], V0r[n], V1r[n], vk, vl16, kvb + (size_t)((rb + S + 1) & 15) * 16384, vmask, bud2);
        if constexpr (S == 8) static_for<4>([&](auto I) { WG_READ(12 + I.value, (12 + I.value) * 8192); });
        if constexpr (S == 14) { GLD4S(g2a, (unsigned)(tid * 32), wsb + WS_MODG + 8192, 0); GLD4S(g2b, (unsigned)(tid * 32), wsb + WS_MODG + 8192, 16); }
        region<1, 1, 1>(s0, s1, pd, o[2], o[3], V1r[c], V0r[c], Kr[n], qf[0], cm);
      } else {
        region<1, 1, 0>(s0, s1, pd, o[2], o[3], V1r[c], V0r[c], V1r[c], qf[0], cm);
      }
      TS(32 + S);
    });
    {
      vmwait<0>(); PIN(g2a); PIN(g2b);
      while (any_lane(g2a[1] != tag || g2a[3] != tag || g2b[1] != tag || g2b[3] != tag) && budget > 0) {
        SLOW_B(); --budget;
        GLD4S(g2a, (unsigned)(tid * 32), wsb + WS_MODG + 8192, 0); GLD4S(g2b, (unsigned)(tid * 32), wsb + WS_MODG + 8192, 16);
        vmwait<0>(); PIN(g2a); PIN(g2b); SLOW_E();
      }
      const int sec = w >> 1, k = (4 * tid) & 511;
      f32x4 v = {__uint_as_float(g2a[0]), __uint_as_float(g2a[2]), __uint_as_float(g2b[0]), __uint_as_float(g2b[2])};
      if (sec == 2) { v[0] = rbf(1.f + v[0]); v[1] = rbf(1.f + v[1]); v[2] = rbf(1.f + v[2]); v[3] = rbf(1.f + v[3]); }
      if (sec == 1 || sec == 2)
        *(LAS u32x2*)(L + L_T2 + (k >> 4) * 64 + ((k >> 2) & 1) * 32 + (sec == 1 ? 16 : 0) + ((k >> 3) & 1) * 8) = (u32x2){pkbf(v[0], v[1]), pkbf(v[2], v[3])};
      if (sec != 1 && (k >> 6) == cb) *(LAS f32x4*)(L + L_TAB + (sec == 0 ? 0 : sec == 2 ? 256 : 512) + (k & 63) * 4) = v;
    }
    {
      cbp xrow = x + (size_t)(2 * tg + (w & 1)) * 32 * 1024, xcol = xrow + cb * 128 + (w >> 1) * 32;
      int l2 = lane; PIN(l2);
      GLD2(xr0, (unsigned)((l2 & 31) * 1024 + (l2 >> 5) * 8), xcol, 0); GLD2(xr1, (unsigned)((l2 & 31) * 1024 + (l2 >> 5) * 8), xcol, 16);
      GLD4(xc, (unsigned)((l2 & 31) * 1024 + (l2 >> 5) * 16), xrow, 0);
    }
    TS(14);
    float f[4][9], mq[4] = {m, m, m, m};
    auto pick = [&]() {
#pragma unroll
      for (int t = 0; t < 4; ++t) {
#pragma unroll
        for (int i = 0; i < 8; ++i) f[t][i] = hd ? o[t][8 + i] : o[t][i];
        f[t][8] = hd ? o[t][0] : o[t][8];
      }
    };
    pick();
    {
      float chk = 0.f; bool bad = false;
#pragma unroll
      for (int t = 0; t < 4; ++t) {
        bad = bad || !(f[t][8] > 1.0e-18f);
#pragma unroll
        for (int i = 0; i < 9; ++i) { chk = fmaxf(chk, fabsf(f[t][i])); bad = bad || (f[t][i] != f[t][i]); }
      }
      bad = bad || !(chk < 1.0e30f);
      if (any_lane(bad)) {
        SLOW_B();
#pragma unroll
        for (int t = 0; t < 4; ++t) { o[t] = zero16(); mq[t] = -3.0e38f; }
        for (int pr = 0; pr < 16; ++pr) {
          cbp pb = kvb + (size_t)pr * 16384;
          u32x4 kk, a, b;
          for (;;) {
            GLD4S(kk, vk, pb, 0); GLD4S(a, vl16, pb, 2048); GLD4S(b, vl16, pb, 3072);
            vmwait3<0>(kk, a, b);
            if (!any_lane(umax3(umax3(kk[0], kk[2], a[0]), umax3(a[2], b[0], b[2]), 0u) == SENT) || budget <= 0) break;
            --budget;
          }
          a = vdata ? a : vc4; b = vdata ? b : vc4;
#pragma unroll
          for (int t = 0; t < 4; ++t) {
            const f32x16 s = MFMA(asb(kk), asb(qf[t]), zero16());
            const float nm = fmaxf(mq[t], xmax32(max16(s)));
            const float al = __builtin_amdgcn_exp2f(mq[t] - nm);
            mq[t] = nm;
            f32x16 pp;
#pragma unroll
            for (int i = 0; i < 16; ++i) { pp[i] = __builtin_amdgcn_exp2f(s[i] - nm); o[t][i] *= al; }
            o[t] = MFMA(asb(a), asb(pack8u(pp, 0)), o[t]); o[t] = MFMA(asb(b), asb(pack8u(pp, 8)), o[t]);
          }
        }
        pick();
        SLOW_E();
      }
    }
    BARRIER();
    static_for<NR2>([&](auto I) { WG_READ(NR1 + I.value, L_RB + I.value * 8192); });
    float fo[10];
#pragma unroll
    for (int i = 0; i < 9; ++i) fo[i] = kq4 == 0 ? f[0][i] : kq4 == 1 ? f[1][i] : kq4 == 2 ? f[2][i] : f[3][i];
    fo[9] = kq4 == 0 ? mq[0] : kq4 == 1 ? mq[1] : kq4 == 2 ? mq[2] : mq[3];
    BARRIER();
#pragma unroll
    for (int t = 0; t < 4; ++t) {
      if (t != kq4) {
        LAS unsigned char* co = L + L_CMB + ((hd * 4 + t) * 3 + ((kq4 - t - 1) & 3)) * 2560;
        *(LAS f32x4*)(co + lane * 16) = (f32x4){f[t][0], f[t][1], f[t][2], f[t][3]};
        *(LAS f32x4*)(co + 1024 + lane * 16) = (f32x4){f[t][4], f[t][5], f[t][6], f[t][7]};
        *(LAS f32x2*)(co + 2048 + lane * 8) = (f32x2){f[t][8], mq[t]};
      }
    }
    BARRIER();
    TS(15);
    f32x4 c0[3], c1[3]; f32x2 c2[3];
#pragma unroll
    for (int d = 0; d < 3; ++d) {
      const LAS unsigned char* ci = L + L_CMB + (w * 3 + d) * 2560;
      c2[d] = *(const LAS f32x2*)(ci + 2048 + lane * 8); c0[d] = *(const LAS f32x4*)(ci + lane * 16); c1[d] = *(const LAS f32x4*)(ci + 1024 + lane * 16);
    }
    const float mm = fmaxf(fmaxf(c2[0][1], c2[1][1]), fmaxf(c2[2][1], fo[9]));
    const float a0 = __builtin_amdgcn_exp2f(c2[0][1] - mm), a1 = __builtin_amdgcn_exp2f(c2[1][1] - mm), a2 = __builtin_amdgcn_exp2f(c2[2][1] - mm), a3 = __builtin_amdgcn_exp2f(fo[9] - mm);
    const float inv = 1.f / ((c2[0][0] * a0 + c2[1][0] * a1) + (c2[2][0] * a2 + fo[8] * a3));
    const float b0 = a0 * inv, b1 = a1 * inv, b2 = a2 * inv, b3 = a3 * inv;
    f32x16 res;
#pragma unroll
    for (int i = 0; i < 4; ++i) {
      res[i] = (c0[0][i] * b0 + c0[1][i] * b1) + (c0[2][i] * b2 + fo[i] * b3);
      res[4 + i] = (c1[0][i] * b0 + c1[1][i] * b1) + (c1[2][i] * b2 + fo[4 + i] * b3);
    }
    GST4S(vl16, clean(pack8u(res, 0)), wsb + WS_O + (size_t)((rb * 4 + kq4) * 32 + 2 * hp + hd) * 1024, 0);
#undef KV_ISSUE
  }
  TS(16);
#define ISSUE_PIECES(V, I0, NP, SB, ROT) static_for<NP>([&](auto I_) { int j_ = I_.value + (ROT); j_ -= j_ >= (NP) ? (NP) : 0; GLD4S(V[(I0) + I_.value], vl16, (SB) + j_ * 1024, 0); })
#define PIN_PIECES(V, I0, NP) static_for<NP>([&](auto I_) { PIN(V[(I0) + I_.value]); })
#define AWAIT_PIECES(V, I0, NP, SB, ROT, NYOUNG) do { \
    vmwait<NYOUNG>(); PIN_PIECES(V, I0, NP); \
    for (;;) { \
      unsigned mx_ = 0; \
      static_for<NP>([&](auto I_) { mx_ = umax3(mx_, V[(I0) + I_.value][0], V[(I0) + I_.value][2]); }); \
      if (!any_lane(mx_ == SENT) || budget <= 0) break; \
      SLOW_B(); --budget; \
      ISSUE_PIECES(V, I0, NP, SB, ROT); \
      vmwait<0>(); PIN_PIECES(V, I0, NP); SLOW_E(); \
    } } while (0)

  {
  int lane_ = lane; PIN(lane_);
  const int lane = lane_, r = lane & 31, h = lane >> 5;
  const unsigned vl16 = lane * 16;
  const unsigned trD = (4 * h + ((lane & 15) >> 2)) * 64 + ((lane >> 4) & 1) * 32 + (lane & 3) * 8;
  const unsigned vwo = (lane >> 2) * 1024 + (lane & 3) * 16;
  const int tt = w & 1, kq = w >> 1;
  const int T35 = 2 * tg + tt, ncol = 32 * (kq >> 1) + 16 * (kq & 1);
  float x1[8];
  constexpr int NG1 = 12;
  LAS f32x4* gbt = (LAS f32x4*)(L + L_GBT + w * 256 + (lane >> 4) * 64);
  const LAS unsigned char* t2p = L + L_T2 + h * 32 + (r >> 4) * 16;
  {
    u32x4 of[8];
    cbp ob = wsb + WS_O + (size_t)(T35 * 32 + 8 * kq) * 1024;
    {
      const LAS unsigned char* wp = L + L_WO + (w >> 1) * 16384 + trD;
#pragma unroll
      for (int i = 0; i < 8; ++i) { const int j = (i + cb) & 7; WO[2 * i] = trfrag<512>(wp + j * 2048); WO[2 * i + 1] = trfrag<512>(wp + j * 2048 + 1024); }
      static_for<16>([&](auto I) { PIN(WO[I.value]); });
    }
    BARRIER();
    ISSUE_PIECES(of, 0, 8, ob, cb);
    {
      f32x16 gbacc = zero16();
#pragma unroll
      for (int i = 0; i < NG1; ++i) gbacc = MFMA(WG[i], *(const LAS bf16x8*)(t2p + i * 64), gbacc);
      if ((lane & 15) == 0) {
#pragma unroll
        for (int i = 0; i < 4; ++i) gbt[i] = (f32x4){gbacc[4 * i], gbacc[4 * i + 1], gbacc[4 * i + 2], gbacc[4 * i + 3]};
      }
      __builtin_amdgcn_sched_barrier(0);
    }
    TS(17);
    AWAIT_PIECES(of, 0, 8, ob, cb, 0);
    PIN(xr0); PIN(xr1); PIN(xc);
    static_for<NR3>([&](auto I) { WG_ISSUE(NR1 + NR2, NR3, I.value, L_RC); });
    TS(18);
    f32x16 acc[2] = {zero16(), zero16()};
#pragma unroll
    for (int i = 0; i < 8; ++i) {
      acc[0] = MFMA(WO[2 * i], asb(of[i]), acc[0]);
      acc[1] = MFMA(WO[2 * i + 1], asb(of[i]), acc[1]);
    }
    f32x4 v0, v1;
#pragma unroll
    for (int p = 0; p < 4; ++p) {
      const int o = 8 * (p & 1);
      const f32x4 d0 = {acc[p >> 1][o], acc[p >> 1][o + 1], acc[p >> 1][o + 2], acc[p >> 1][o + 3]};
      const f32x4 d1 = {acc[p >> 1][o + 4], acc[p >> 1][o + 5], acc[p >> 1][o + 6], acc[p >> 1][o + 7]};
      if (p == kq) { v0 = d0; v1 = d1; }
      else {
        LAS f32x4* d = (LAS f32x4*)(L + L_PS + ((2 * p + tt) * 3 + ((p ^ kq) - 1)) * 2048 + lane * 16);
        d[0] = d0; d[64] = d1;
      }
    }
    float ct = dot2bf(xc[0], 0x3f803f80u, 0.f); ct = dot2bf(xc[1], 0x3f803f80u, ct); ct = dot2bf(xc[2], 0x3f803f80u, ct); ct = dot2bf(xc[3], 0x3f803f80u, ct);
    ct = xsum32(ct) * (1.f / 16.f);
    const LAS f32x4* tab = (const LAS f32x4*)(L + L_TAB + (ncol + 4 * h) * 4);
    const f32x4 ga0 = tab[0], ga1 = tab[2], g20 = tab[16], g21 = tab[18];
    vmwait<0>();
    BARRIER();
    TS(19);
    static_for<NR3>([&](auto I) { WG_READ(NR1 + NR2 + I.value, L_RC + I.value * 8192); });
    const LAS f32x4* si = (const LAS f32x4*)(L + L_PS + w * 6144 + lane * 16);
    v0 = (v0 + si[0]) + (si[128] + si[256]); v1 = (v1 + si[64]) + (si[192] + si[320]);
    const float xv[8] = {bflo(xr0[0]), bfhi(xr0[0]), bflo(xr0[1]), bfhi(xr0[1]), bflo(xr1[0]), bfhi(xr1[0]), bflo(xr1[1]), bfhi(xr1[1])};
    f32x16 y; float s1 = 0.f, s2 = 0.f;
#pragma unroll
    for (int j = 0; j < 8; ++j) {
      x1[j] = xv[j] + (j < 4 ? ga0[j & 3] * v0[j & 3] : ga1[j & 3] * v1[j & 3]);
      const float d = x1[j] - ct;
      s1 += d; s2 += d * d;
      y[j] = d * (j < 4 ? g20[j & 3] : g21[j & 3]);
    }
    GST4S(vl16, clean(pack8u(y, 0)), wsb + WS_Y + (size_t)(T35 * 32 + 4 * cb + kq) * 1024, 0);
    s1 = xsum32(s1); s2 = xsum32(s2);
    if (lane < 32) *(LAS f32x2*)(L + L_ST3 + ((tt * 4 + kq) * 32 + r) * 8) = (f32x2){s1, s2};
    BARRIER();
    if (w < 2 && lane < 32) {
      const LAS f32x2* sp = (const LAS f32x2*)(L + L_ST3 + (tt * 128 + r) * 8);
      const f32x2 t = (sp[0] + sp[32]) + (sp[64] + sp[96]);
      const u32x2 g = {min(__float_as_uint(t[0]), SENT - 1u), __float_as_uint(t[1])};
      GST2S((unsigned)(r * 8), g, wsb + WS_ST + (size_t)(T35 * 8 + cb) * 256, 0);
    }
  }
  TS(20);
  const int k0 = kq < 2 ? 22 * kq : 44 + 21 * (kq - 2);
  cbp wdb = wd + cb * 128 + tt * 64;
#define WD_ISSUE(I0, NI, LBASE, PER) static_for<NI>([&](auto I_) { int i_ = I_.value + ((rot4 * (NI)) >> 2); i_ -= i_ >= (NI) ? (NI) : 0; \
    glds_s(vwo, wdb + (size_t)min(k0 + (I0) + i_, NU - 1) * 16384, lb + (LBASE) + (kq * (PER) + 2 * i_ + tt) * 1024); })
  bf16x8 WA[14];
  const int r7 = cb == 7 ? 0 : cb, r6 = cb >= 6 ? cb - 6 : cb;
  {
    const int Tb = 3 * tg4;
    u32x4 ys[12]; u32x2 st[4];
    const int wy = (w + ug) & 7;
    cbp yb0 = wsb + WS_Y + (size_t)(Tb * 32 + 4 * wy) * 1024;
    cbp yb1 = wsb + WS_Y + (size_t)(min(Tb + 1, 63) * 32 + 4 * wy) * 1024, yb2 = wsb + WS_Y + (size_t)(min(Tb + 2, 63) * 32 + 4 * wy) * 1024;
    cbp stb = wsb + WS_ST + (size_t)(min(Tb + min(w, 2), 63) * 8) * 256;
    const unsigned vst = h * 1024 + r * 8;
    if (act4) {
      ISSUE_PIECES(ys, 0, 4, yb0, 0);
      f32x16 gbacc;
#pragma unroll
      for (int i = 0; i < 4; ++i) { const f32x4 t = gbt[i]; gbacc[4 * i] = t[0]; gbacc[4 * i + 1] = t[1]; gbacc[4 * i + 2] = t[2]; gbacc[4 * i + 3] = t[3]; }
#pragma unroll
      for (int i = NG1; i < 32; ++i) gbacc = MFMA(WG[i], *(const LAS bf16x8*)(t2p + i * 64), gbacc);
      if ((lane & 15) == 0) {
#pragma unroll
        for (int i = 0; i < 4; ++i) gbt[i] = (f32x4){gbacc[4 * i], gbacc[4 * i + 1], gbacc[4 * i + 2], gbacc[4 * i + 3]};
      }
      TS(21);
      LAS u32x4* yd = (LAS u32x4*)(L + L_Y + wy * 4096 + lane * 16);
      AWAIT_PIECES(ys, 0, 4, yb0, 0, 0);
      GLD2S(st[0], vst, stb, 0); GLD2S(st[1], vst, stb, 256); GLD2S(st[2], vst, stb, 512); GLD2S(st[3], vst, stb, 768);
      ISSUE_PIECES(ys, 4, 4, yb1, 0); ISSUE_PIECES(ys, 8, 4, yb2, 0);
#pragma unroll
      for (int i = 0; i < 4; ++i) yd[i * 64] = ys[i];
      BARRIER();
      TS(22);
      WD_ISSUE(0, 7, L_RA, 14);
      const LAS unsigned char* yl = L + L_Y + lane * 16;
      cbp hb = wsb + WS_HH + (size_t)(Tb * NU + unit) * 1024;
      auto tile_mma = [&](int t) {
        f32x16 a = zero16();
#pragma unroll
        for (int i = 0; i < 32; ++i) a = MFMA(WG[i], *(const LAS bf16x8*)(yl + t * 32768 + i * 1024), a);
        return a;
      };
      auto tile_out = [&](int t, const f32x16& a) {
        const f32x2 sr = *(const LAS f32x2*)(L + L_STT + (t * 32 + r) * 8);
        const LAS f32x4* gb = (const LAS f32x4*)(L + L_GBT + w * 256 + h * 128);
        float z[16];
#pragma unroll
        for (int i = 0; i < 4; ++i) {
          const f32x4 G = gb[i], B = gb[4 + i];
#pragma unroll
          for (int e = 0; e < 4; ++e) z[4 * i + e] = a[4 * i + e] * sr[0] + (sr[1] * G[e] + B[e]);
        }
        f32x16 hv;
#pragma unroll
        for (int j = 0; j < 8; ++j) hv[j] = z[8 + j] * __builtin_amdgcn_rcpf(1.f + __builtin_amdgcn_exp2f(-LOG2E * z[j]));
        GST4S(vl16, clean(pack8u(hv, 0)), hasu && t < nt4 ? hb + (size_t)t * NU * 1024 : wsb + WS_DUMP + (size_t)(bx * 8 + w) * 1024, 0);
      };
      f32x16 a = tile_mma(0);
      PIN(a);
      TS(23);
      AWAIT_PIECES(ys, 4, 4, yb1, 0, 11);
#pragma unroll
      for (int i = 0; i < 4; ++i) yd[2048 + i * 64] = ys[4 + i];
      AWAIT_PIECES(ys, 8, 4, yb2, 0, 7);
#pragma unroll
      for (int i = 0; i < 4; ++i) yd[4096 + i * 64] = ys[8 + i];
      if (w < 3) {
        vmwait<7>(); PIN_PIECES(st, 0, 4);
        while (any_lane(max(umax3(st[0][0], st[1][0], st[2][0]), st[3][0]) == SENT) && budget > 0) {
          SLOW_B(); --budget;
          GLD2S(st[0], vst, stb, 0); GLD2S(st[1], vst, stb, 256); GLD2S(st[2], vst, stb, 512); GLD2S(st[3], vst, stb, 768);
          vmwait<0>(); PIN_PIECES(st, 0, 4); SLOW_E();
        }
        const float s1 = xsum32((__uint_as_float(st[0][0]) + __uint_as_float(st[1][0])) + (__uint_as_float(st[2][0]) + __uint_as_float(st[3][0])));
        const float s2 = xsum32((__uint_as_float(st[0][1]) + __uint_as_float(st[1][1])) + (__uint_as_float(st[2][1]) + __uint_as_float(st[3][1])));
        const float dl = s1 * (1.f / 512.f), var = fmaxf(s2 * (1.f / 512.f) - dl * dl, 0.f), rstd = 1.f / sqrtf(var + LN_EPS);
        if (lane < 32) *(LAS f32x2*)(L + L_STT + (w * 32 + r) * 8) = (f32x2){rstd, -dl * rstd};
      }
      BARRIER();
      WD_ISSUE(7, 4, 0, 8);
      tile_out(0, a);
      TS(24);
      a = tile_mma(1);
      vmwait<5>();
      BARRIER();
      WD_ISSUE(11, 4, 32768, 8);
      tile_out(1, a);
      TS(25);
      a = tile_mma(2);
      tile_out(2, a);
      TS(26);
      vmwait<2>();
    } else {
      WD_ISSUE(0, 7, L_RA, 14); WD_ISSUE(7, 4, 0, 8); WD_ISSUE(11, 4, 32768, 8);
      vmwait<0>();
      BARRIER();
    }
#pragma unroll
    for (int i = 0; i < 7; ++i) {
      int j = i + r7; j -= j >= 7 ? 7 : 0;
      WA[2 * i] = trfrag<512>(L + L_RA + (kq * 14 + 2 * j) * 1024 + trD); WA[2 * i + 1] = trfrag<512>(L + L_RA + (kq * 14 + 2 * j + 1) * 1024 + trD);
    }
  }
  TS(27);
  {
    u32x4 hf[22];
    cbp hb = wsb + WS_HH + (size_t)(T35 * NU + k0) * 1024;
    cbp hb2 = hb + 15 * 1024, hb3 = hb + (kq < 2 ? 21 : 20) * 1024;
    BARRIER();
    ISSUE_PIECES(hf, 0, 7, hb, r7); ISSUE_PIECES(hf, 7, 8, hb + 7168, cb); ISSUE_PIECES(hf, 15, 6, hb2, r6); ISSUE_PIECES(hf, 21, 1, hb3, 0);
    WD_ISSUE(15, 7, L_WC, 14);
    const LAS f32x4* tab = (const LAS f32x4*)(L + L_TAB + 512 + (ncol + 4 * h) * 4);
    const f32x4 gb0 = tab[0], gb1 = tab[2];
    f32x16 acc[2] = {zero16(), zero16()};
    AWAIT_PIECES(hf, 0, 7, hb, r7, 22);
    TS(28);
#pragma unroll
    for (int i = 0; i < 7; ++i) { acc[0] = MFMA(WA[2 * i], asb(hf[i]), acc[0]); acc[1] = MFMA(WA[2 * i + 1], asb(hf[i]), acc[1]); }
    AWAIT_PIECES(hf, 7, 8, hb + 7168, cb, 14);
    TS(29);
#pragma unroll
    for (int i = 0; i < 8; ++i) {
      const int j = (i + cb) & 7;
      const LAS unsigned char* wp = L + (j >> 2) * 32768 + (kq * 8 + 2 * (j & 3)) * 1024 + trD;
      acc[0] = MFMA(trfrag<512>(wp), asb(hf[7 + i]), acc[0]); acc[1] = MFMA(trfrag<512>(wp + 1024), asb(hf[7 + i]), acc[1]);
    }
    AWAIT_PIECES(hf, 15, 6, hb2, r6, 0);
    AWAIT_PIECES(hf, 21, 1, hb3, 0, 0);
    BARRIER();
    TS(30);
#pragma unroll
    for (int i = 0; i < 7; ++i) {
      if (i < 6 || kq < 2) {
        int j = i + r6; j = i == 6 ? 6 : j >= 6 ? j - 6 : j;
        const LAS unsigned char* wp = L + L_WC + (kq * 14 + 2 * j) * 1024 + trD;
        acc[0] = MFMA(trfrag<512>(wp), asb(hf[15 + i]), acc[0]); acc[1] = MFMA(trfrag<512>(wp + 1024), asb(hf[15 + i]), acc[1]);
      }
    }
#pragma unroll
    for (int p = 0; p < 4; ++p) {
      LAS f32x4* d = (LAS f32x4*)(L + (2 * p + tt) * 8192 + kq * 2048 + lane * 16);
      const int o = 8 * (p & 1);
      d[0] = (f32x4){acc[p >> 1][o], acc[p >> 1][o + 1], acc[p >> 1][o + 2], acc[p >> 1][o + 3]};
      d[64] = (f32x4){acc[p >> 1][o + 4], acc[p >> 1][o + 5], acc[p >> 1][o + 6], acc[p >> 1][o + 7]};
    }
    BARRIER();
    TS(31);
    const LAS f32x4* si = (const LAS f32x4*)(L + w * 8192 + lane * 16);
    const f32x4 v0 = (si[0] + si[128]) + (si[256] + si[384]), v1 = (si[64] + si[192]) + (si[320] + si[448]);
    const unsigned a0 = pkbf(x1[0] + gb0[0] * v0[0], x1[1] + gb0[1] * v0[1]), a1 = pkbf(x1[2] + gb0[2] * v0[2], x1[3] + gb0[3] * v0[3]);
    const unsigned b0 = pkbf(x1[4] + gb1[0] * v1[0], x1[5] + gb1[1] * v1[1]), b1 = pkbf(x1[6] + gb1[2] * v1[2], x1[7] + gb1[3] * v1[3]);
    auto t0 = __builtin_amdgcn_permlane32_swap(a0, b0, false, false), t1 = __builtin_amdgcn_permlane32_swap(a1, b1, false, false);
    const u32x4 ov = {t0[0], t1[0], t0[1], t1[1]};
    *(u32x4*)(out + (size_t)(T35 * 32 + r) * 512 + cb * 64 + ncol + 8 * h) = ov;
  }
  }
  TS_END();
  asm volatile(".Lcode1:");
}

extern "C" void kernel_launch(void* const* d_in, const int* in_sizes, int n_in, void* d_out, int out_size, void* d_ws, size_t ws_size,
                              hipStream_t stream) {
  fused_block<<<256, 512, 0, stream>>>((const u16*)d_in[0], (const u16*)d_in[1], (const u16*)d_in[2], (const u16*)d_in[4], (const u16*)d_in[5],
                                       (const u16*)d_in[6], (const u16*)d_in[7], (const u16*)d_in[8], (unsigned char*)d_ws, (u16*)d_out);
}
```
